```python
import torch

def _softmax(x, axis=-1):
    return torch.softmax(x, dim=axis)

def _gelu(x):
    return torch.nn.functional.gelu(x, approximate='tanh')

def _silu(x):
    return torch.nn.functional.silu(x)

def _sigmoid(x):
    return torch.sigmoid(x)

def _layernorm(x, axis=-1, eps=1e-05):
    m = x.mean(dim=axis, keepdim=True)
    v = x.var(dim=axis, keepdim=True, unbiased=False)
    return (x - m) / torch.sqrt(v + eps)

def _scatter_add(tgt, idx, src, axis=0):
    return tgt.clone().index_add_(axis, idx, src)

def _segment_sum(src, seg, n):
    out = torch.zeros((n,) + src.shape[1:], dtype=src.dtype)
    return out.index_add_(0, seg, src)

def _pad(x, pw):
    flat = tuple((v for pair in reversed(pw) for v in pair))
    return torch.nn.functional.pad(x, flat)
kernel_name = 'adaLN_cond__sliding_block_cached__glu_gated_geglu__decode'

def setup_inputs():

    _w = lambda a: (lambda t: t.to(torch.bfloat16) if t.is_floating_point() else t)(torch.as_tensor(a))

    def _A_setup():
        B, Sq, D = (1, 1, 512)
        Hq, Hkv, Sk, Dh = (32, 8, 65536, 128)
        assert Hq % Hkv == 0, (Hq, Hkv)
        return {'x': _w((lambda *s: torch.randn(*s, dtype=torch.bfloat16) * 0.05)(B, Sq, D)), 'k_cache': _w((lambda *s: torch.randn(*s, dtype=torch.bfloat16) * 0.8)(B, Hkv, Sk, Dh)), 'v_cache': _w((lambda *s: torch.randn(*s, dtype=torch.bfloat16) * 3.2)(B, Hkv, Sk, Dh)), 'wq': _w((lambda *s: torch.randn(*s, dtype=torch.bfloat16) * 0.8)(D, Hq * Dh) * D ** (-0.5)), 'wo': _w((lambda *s: torch.randn(*s, dtype=torch.bfloat16) * 3.2)(Hq * Dh, D) * (Hq * Dh) ** (-0.5))}
    _A_in = _A_setup()

    def _B_setup():
        B, S, Dm, Df = (1, 1, 512, 1792)
        return {'x': _w((lambda *s: torch.randn(*s, dtype=torch.bfloat16) * 0.05)(B, S, Dm)), 'w_gate': _w((lambda *s: torch.randn(*s, dtype=torch.bfloat16) * 3.2)(Dm, Df) * Dm ** (-0.5)), 'w_up': _w((lambda *s: torch.randn(*s, dtype=torch.bfloat16) * 3.2)(Dm, Df) * Dm ** (-0.5)), 'w_down': _w((lambda *s: torch.randn(*s, dtype=torch.bfloat16) * 3.2)(Df, Dm) * Df ** (-0.5))}
    _B_in = _B_setup()
    return {'A_x': _A_in['x'], 'A_k_cache': _A_in['k_cache'], 'A_v_cache': _A_in['v_cache'], 'A_wq': _A_in['wq'], 'A_wo': _A_in['wo'], 'B_x': _B_in['x'], 'B_w_gate': _B_in['w_gate'], 'B_w_up': _B_in['w_up'], 'B_w_down': _B_in['w_down'], 'c': _w((lambda *s: torch.randn(*s, dtype=torch.bfloat16) * 0.4)(1, 512) * 8.0), 'w_mod': _w((lambda *s: torch.randn(*s, dtype=torch.bfloat16) * 0.4)(512, 6 * 512) * 512 ** (-0.5) * 8.0)}

def reference(A_x, A_k_cache, A_v_cache, A_wq, A_wo, B_x, B_w_gate, B_w_up, B_w_down, c, w_mod):

    def _A_ref(x, k_cache, v_cache, wq, wo):
        B, Sq, D = x.shape
        Hq, Dh = (32, 128)
        Hkv, Sk = (k_cache.shape[1], k_cache.shape[2])
        W = min(4096, max(256, Sk // 4))
        rep = Hq // Hkv
        q = (lambda x, *d: x.permute(*d))((x @ wq).reshape(B, Sq, Hq, Dh), 0, 2, 1, 3)
        k = (lambda x, n, axis: torch.repeat_interleave(x, n, dim=axis))(k_cache[:, :, Sk - W:, :], rep, axis=1)
        v = (lambda x, n, axis: torch.repeat_interleave(x, n, dim=axis))(v_cache[:, :, Sk - W:, :], rep, axis=1)
        scores = torch.einsum('bhqd,bhkd->bhqk', q, k) * Dh ** (-0.5)
        p = _softmax(scores, axis=-1)
        o = torch.einsum('bhqk,bhkd->bhqd', p, v)
        return (lambda x, *d: x.permute(*d))(o, 0, 2, 1, 3).reshape(B, Sq, Hq * Dh) @ wo

    def _B_ref(x, w_gate, w_up, w_down):
        g = torch.einsum('bsd,df->bsf', x, w_gate)
        u = torch.einsum('bsd,df->bsf', x, w_up)
        h = _gelu(g) * u
        return torch.einsum('bsf,fd->bsd', h, w_down)
    x = A_x
    mod = (_silu(c) @ w_mod).reshape(c.shape[0], 6, 1, -1)
    sa, ca, ga, sb, cb, gb = (mod[:, 0].clone(), mod[:, 1].clone(), mod[:, 2].clone(), mod[:, 3].clone(), mod[:, 4].clone(), mod[:, 5].clone())
    h = _layernorm(x, axis=-1) * (1 + ca) + sa
    x = x + ga * _A_ref(h, A_k_cache, A_v_cache, A_wq, A_wo)
    h = _layernorm(x, axis=-1) * (1 + cb) + sb
    x = x + gb * _B_ref(h, B_w_gate, B_w_up, B_w_down)
    return x
```

```cpp
#include <hip/hip_runtime.h>
#include <hip/hip_bf16.h>
#include <stdint.h>

typedef unsigned short u16;
#define MIMIC 1
__device__ __forceinline__ float bf2f(u16 v) { return __uint_as_float(((unsigned)v) << 16); }
__device__ __forceinline__ float rbf(float f) {
    return __bfloat162float(__float2bfloat16(f));
}
__device__ __forceinline__ float blk_sum(float v, float* red) {
    for (int o = 32; o > 0; o >>= 1) v += __shfl_xor(v, o);
    int w = threadIdx.x >> 6;
    __syncthreads();
    if ((threadIdx.x & 63) == 0) red[w] = v;
    __syncthreads();
    float s = 0; for (int i = 0; i < (int)(blockDim.x >> 6); ++i) s += red[i];
    return s;
}
__device__ __forceinline__ float gemv_col(const float* xs, const u16* W, int K, int N, int col, float* red) {
    int w = threadIdx.x >> 6;
    float acc = 0;
    for (int k = w; k < K; k += 4) acc += xs[k] * bf2f(W[(size_t)k * N + col]);
    __syncthreads();
    red[threadIdx.x] = acc;
    __syncthreads();
    int l = threadIdx.x & 63;
    return red[l] + red[l + 64] + red[l + 128] + red[l + 192];
}
__global__ void k_mod(const u16* c, const u16* wmod, float* mod) {
    __shared__ float xs[512]; __shared__ float red[256];
    for (int i = threadIdx.x; i < 512; i += 256) { float v = bf2f(c[i]); xs[i] = rbf(v / (1.f + __expf(-v))); }
    __syncthreads();
    int col = blockIdx.x * 64 + (threadIdx.x & 63);
    float r = gemv_col(xs, wmod, 512, 3072, col, red);
    if (threadIdx.x < 64) mod[col] = rbf(r);
}
__device__ void ln_mod(const float* x, const float* sh, const float* sc, float* h, float* red) {
    float a = x[threadIdx.x], b = x[threadIdx.x + 256];
    float m = blk_sum(a + b, red) * (1.f / 512.f);
    float v = blk_sum((a - m) * (a - m) + (b - m) * (b - m), red) * (1.f / 512.f);
    m = rbf(m); v = rbf(v);
    float d = rbf(sqrtf(rbf(v + 1e-5f)));
    for (int j = 0; j < 2; ++j) {
        int i = threadIdx.x + 256 * j;
        float xv = j ? b : a;
        float n = rbf(rbf(xv - m) / d);
        h[i] = rbf(rbf(n * rbf(1.f + sc[i])) + sh[i]);
    }
    __syncthreads();
}
__global__ void k_q(const u16* x, const float* mod, const u16* wq, float* q) {
    __shared__ float xs[512]; __shared__ float hs[512]; __shared__ float red[256];
    for (int i = threadIdx.x; i < 512; i += 256) xs[i] = bf2f(x[i]);
    __syncthreads();
    ln_mod(xs, mod, mod + 512, hs, red);
    int col = blockIdx.x * 64 + (threadIdx.x & 63);
    float r = gemv_col(hs, wq, 512, 4096, col, red);
    if (threadIdx.x < 64) q[col] = rbf(r);
}
__global__ void k_attn(const float* q, const u16* kc, const u16* vc, float* o) {
    __shared__ float qs[128]; __shared__ float sc[4096]; __shared__ float red[256];
    int h = blockIdx.x, g = h >> 2;
    if (threadIdx.x < 128) qs[threadIdx.x] = q[h * 128 + threadIdx.x];
    __syncthreads();
    const u16* K = kc + ((size_t)g * 65536 + (65536 - 4096)) * 128;
    const u16* V = vc + ((size_t)g * 65536 + (65536 - 4096)) * 128;
    float mx = -INFINITY;
    for (int k = threadIdx.x; k < 4096; k += 256) {
        float a = 0;
        for (int d = 0; d < 128; ++d) a += qs[d] * bf2f(K[(size_t)k * 128 + d]);
        a = rbf(rbf(a) * 0.08838834764831845f);
        sc[k] = a; mx = fmaxf(mx, a);
    }
    for (int of = 32; of > 0; of >>= 1) mx = fmaxf(mx, __shfl_xor(mx, of));
    __syncthreads();
    if ((threadIdx.x & 63) == 0) red[threadIdx.x >> 6] = mx;
    __syncthreads();
    mx = fmaxf(fmaxf(red[0], red[1]), fmaxf(red[2], red[3]));
    float s = 0;
    for (int k = threadIdx.x; k < 4096; k += 256) { float e = __expf(sc[k] - mx); sc[k] = e; s += e; }
    s = blk_sum(s, red);
    float inv = 1.f / s;
    __syncthreads();
    int d = threadIdx.x & 127, half = threadIdx.x >> 7;
    float acc = 0;
    for (int k = half; k < 4096; k += 2) acc += rbf(sc[k] * inv) * bf2f(V[(size_t)k * 128 + d]);
    __syncthreads();
    red[threadIdx.x] = acc;
    __syncthreads();
    if (threadIdx.x < 128) o[h * 128 + threadIdx.x] = rbf(red[threadIdx.x] + red[threadIdx.x + 128]);
}
__global__ void k_wo(const u16* x, const float* mod, const float* o, const u16* wo, float* x2) {
    __shared__ float os[4096]; __shared__ float red[256];
    for (int i = threadIdx.x; i < 4096; i += 256) os[i] = o[i];
    __syncthreads();
    int col = blockIdx.x * 64 + (threadIdx.x & 63);
    float r = gemv_col(os, wo, 4096, 512, col, red);
    if (threadIdx.x < 64) x2[col] = rbf(bf2f(x[col]) + rbf(mod[1024 + col] * rbf(r)));
}
__global__ void k_gu(const float* x2, const float* mod, const u16* wg, const u16* wu, float* hh) {
    __shared__ float hs[512]; __shared__ float red[256];
    ln_mod(x2, mod + 1536, mod + 2048, hs, red);
    int col = blockIdx.x * 64 + (threadIdx.x & 63);
    float g = rbf(gemv_col(hs, wg, 512, 1792, col, red));
    float u = rbf(gemv_col(hs, wu, 512, 1792, col, red));
    if (threadIdx.x < 64) {
        float t = 0.7978845608028654f * (g + 0.044715f * g * g * g);
        float ge = rbf(0.5f * g * (1.f + tanhf(t)));
        hh[col] = rbf(ge * u);
    }
}
__global__ void k_down(const float* x2, const float* mod, const float* hh, const u16* wd, __hip_bfloat16* out) {
    __shared__ float hs[1792]; __shared__ float red[256];
    for (int i = threadIdx.x; i < 1792; i += 256) hs[i] = hh[i];
    __syncthreads();
    int col = blockIdx.x * 64 + (threadIdx.x & 63);
    float r = gemv_col(hs, wd, 1792, 512, col, red);
    if (threadIdx.x < 64) out[col] = __float2bfloat16(x2[col] + rbf(mod[2560 + col] * rbf(r)));
}

extern "C" void kernel_launch(void* const* d_in, const int* in_sizes, int n_in,
                              void* d_out, int out_size, void* d_ws, size_t ws_size,
                              hipStream_t stream) {
    const u16* x = (const u16*)d_in[0]; const u16* kc = (const u16*)d_in[1]; const u16* vc = (const u16*)d_in[2];
    const u16* wq = (const u16*)d_in[3]; const u16* wo = (const u16*)d_in[4];
    const u16* wg = (const u16*)d_in[6]; const u16* wu = (const u16*)d_in[7]; const u16* wd = (const u16*)d_in[8];
    const u16* c = (const u16*)d_in[9]; const u16* wmod = (const u16*)d_in[10];
    float* ws = (float*)d_ws;
    float* mod = ws; float* q = ws + 4096; float* o = ws + 8192; float* x2 = ws + 12288; float* hh = ws + 16384;
    k_mod<<<48, 256, 0, stream>>>(c, wmod, mod);
    k_q<<<64, 256, 0, stream>>>(x, mod, wq, q);
    k_attn<<<32, 256, 0, stream>>>(q, kc, vc, o);
    k_wo<<<8, 256, 0, stream>>>(x, mod, o, wo, x2);
    k_gu<<<28, 256, 0, stream>>>(x2, mod, wg, wu, hh);
    k_down<<<8, 256, 0, stream>>>(x2, mod, hh, wd, (__hip_bfloat16*)d_out);
}
```

```cpp
#include <hip/hip_runtime.h>
#include <stdint.h>

typedef unsigned u32;
typedef unsigned short u16;
typedef u32 v4u __attribute__((ext_vector_type(4)));
typedef u32 v2u __attribute__((ext_vector_type(2)));
typedef float v4f __attribute__((ext_vector_type(4)));
typedef float v2f __attribute__((ext_vector_type(2)));
typedef short v4s __attribute__((ext_vector_type(4)));
typedef __bf16 v8bf __attribute__((ext_vector_type(8)));
typedef __bf16 v2bf __attribute__((ext_vector_type(2)));
#define GAS __attribute__((address_space(1)))

#define DLY_A 0
#define DLY_WO 0

constexpr u32 OFF_EP = 0;
constexpr u32 OFF_XH = 4096;
constexpr u32 OFF_XM = 8192;
constexpr u32 OFF_XQ = 16384;
constexpr u32 OFF_XS = 32768;
constexpr u32 OFF_XW = 49152;
constexpr u32 OFF_XA = 131072;
constexpr u32 OFF_XD = 393216;
constexpr u32 OFF_DBG = 1u << 20;
constexpr int SPIN_MAX = 1 << 16;

__device__ __forceinline__ float asf(u32 v) { return __uint_as_float(v); }
__device__ __forceinline__ u32 asu(float v) { return __float_as_uint(v); }
__device__ __forceinline__ float lo16(u32 v) { return asf(v << 16); }
__device__ __forceinline__ float hi16(u32 v) { return asf(v & 0xffff0000u); }
__device__ __forceinline__ u32 pk2(float a, float b) { v2f t = {a, b}; v2bf r = __builtin_convertvector(t, v2bf); return __builtin_bit_cast(u32, r); }
__device__ __forceinline__ float rbf(float a) { return lo16(pk2(a, a) & 0xffffu); }
__device__ __forceinline__ v4u ldnt(const void* p) { return __builtin_nontemporal_load((const GAS v4u*)p); }
__device__ __forceinline__ void st16(void* p, v4u v) { asm volatile("global_store_dwordx4 %0, %1, off sc1\n s_nop 1" : : "v"(p), "v"(v) : "memory"); }
__device__ __forceinline__ v4u ld16(const void* p) { v4u v; asm volatile("global_load_dwordx4 %0, %1, off sc1" : "=v"(v) : "v"(p) : "memory"); return v; }
__device__ __forceinline__ void bar() { asm volatile("s_waitcnt lgkmcnt(0)\n s_barrier" ::: "memory"); }
template <int C> __device__ __forceinline__ float dpp(float x) { return asf(__builtin_amdgcn_update_dpp(0, asu(x), C, 0xf, 0xf, true)); }
#define QP1 0xB1
#define QP2 0x4E
#define ROR(n) (0x120 + (n))
__device__ __forceinline__ float x16add(float x) { auto r = __builtin_amdgcn_permlane16_swap(asu(x), asu(x), false, false); return asf(r[0]) + asf(r[1]); }
__device__ __forceinline__ float x32add(float x) { auto r = __builtin_amdgcn_permlane32_swap(asu(x), asu(x), false, false); return asf(r[0]) + asf(r[1]); }
__device__ __forceinline__ float x16max(float x) { auto r = __builtin_amdgcn_permlane16_swap(asu(x), asu(x), false, false); return fmaxf(asf(r[0]), asf(r[1])); }
__device__ __forceinline__ float x32max(float x) { auto r = __builtin_amdgcn_permlane32_swap(asu(x), asu(x), false, false); return fmaxf(asf(r[0]), asf(r[1])); }

template <int N> __device__ __forceinline__ void gather(v4u (&v)[N], const char* const (&p)[N], u32 tag) {
    for (int spin = 0; spin < SPIN_MAX; ++spin) {
#pragma unroll
        for (int i = 0; i < N; ++i) v[i] = ld16(p[i]);
        asm volatile("s_waitcnt vmcnt(0)" ::: "memory");
        u32 bad = 0;
#pragma unroll
        for (int i = 0; i < N; ++i) { asm volatile("" : "+v"(v[i])); bad |= (v[i].y ^ tag) | (v[i].w ^ tag); }
        if (__all(bad == 0)) break;
    }
}
__device__ __forceinline__ void perm4(v2u (&op)[8], const v4u (&r)[4]) {
#pragma unroll
    for (int j = 0; j < 8; ++j) {
        const u32 sel = (j & 1) ? 0x07060302u : 0x05040100u;
        op[j].x = __builtin_amdgcn_perm(r[1][j >> 1], r[0][j >> 1], sel);
        op[j].y = __builtin_amdgcn_perm(r[3][j >> 1], r[2][j >> 1], sel);
    }
}
__device__ __forceinline__ v4f mfma16(v2u a, v2u b) {
    return __builtin_amdgcn_mfma_f32_16x16x16bf16_1k(__builtin_bit_cast(v4s, a), __builtin_bit_cast(v4s, b), v4f{0.f, 0.f, 0.f, 0.f}, 0, 0, 0);
}
__device__ __forceinline__ v4f mfma32(v4u a, v4u b, v4f c) {
    return __builtin_amdgcn_mfma_f32_16x16x32_bf16(__builtin_bit_cast(v8bf, a), __builtin_bit_cast(v8bf, b), c, 0, 0, 0);
}
__device__ __forceinline__ void gemv32(float* red, v2u a, const v2u (&op)[8], int w, int l) {
    v4f d[8];
#pragma unroll
    for (int j = 0; j < 8; ++j) d[j] = mfma16(a, op[j]);
    if (((l >> 2) & 3) == (l >> 4)) {
        float* q = red + (8 * (l & 3)) * 36 + 4 * w + (l >> 4);
#pragma unroll
        for (int j = 0; j < 8; ++j) q[j * 36] = d[j][0];
    }
}
__device__ __forceinline__ float red32(const float* red, int l) {
    const v4f* q = (const v4f*)(red + (l & 31) * 36 + 16 * (l >> 5));
    v4f s = (q[0] + q[1]) + (q[2] + q[3]);
    return x32add((s[0] + s[1]) + (s[2] + s[3]));
}
__device__ __forceinline__ void pub_bf16(char* line, float r, u32 tag, int l) {
    float r1 = dpp<QP1>(r);
    u32 p = pk2(r, r1);
    u32 p2 = __builtin_amdgcn_update_dpp(0, p, QP2, 0xf, 0xf, true);
    if (l < 32 && (l & 3) == 0) st16(line + l * 4, v4u{p, tag, p2, tag});
}
__device__ __forceinline__ float silu_bf(float v) { return v * __builtin_amdgcn_rcpf(1.f + __expf(-v)); }

#define TS(i)
#define TSDUMP()

__global__ void __launch_bounds__(576) fused_kernel(const u16* __restrict__ xin, const u16* __restrict__ kc, const u16* __restrict__ vc,
        const u16* __restrict__ wq, const u16* __restrict__ wo, const u16* __restrict__ wg, const u16* __restrict__ wu,
        const u16* __restrict__ wd, const u16* __restrict__ cin, const u16* __restrict__ wmod, u16* __restrict__ out, char* __restrict__ ws) {
    __shared__ __attribute__((aligned(16))) float red0[32 * 36];
    __shared__ __attribute__((aligned(16))) float red1[32 * 36];
    __shared__ __attribute__((aligned(16))) float red2[32 * 36];
    __shared__ __attribute__((aligned(16))) v4f po[8 * 128];
    __shared__ __attribute__((aligned(16))) u32 qs[256];
    __shared__ __attribute__((aligned(16))) float st[64];
    __shared__ __attribute__((aligned(16))) float wt[64];
    __shared__ __attribute__((aligned(16))) float lns[32];
    __shared__ __attribute__((aligned(16))) u32 hhs[16];
    const int b = blockIdx.x, tid = threadIdx.x, l = tid & 63;
    const int w = __builtin_amdgcn_readfirstlane(tid >> 6);
    const int xcd = b & 7, idx = b >> 3;
    TS(0);
    const u32 epoch = __hip_atomic_load((const GAS u32*)(ws + OFF_EP), __ATOMIC_RELAXED, __HIP_MEMORY_SCOPE_AGENT);
    const int r4 = 64 * w + (l & 60);
    const u32 wofs = (u32)(l & 3) * 16;

    if (idx >= 16) {
        const int c = idx - 16, g = (xcd + 1) & 7;
        if (w < 8) {
            v4u rm[4], rq[4]; v2u cv;
            const bool ismod = c < 4;
            if (ismod) {
                cv = *(const GAS v2u*)(cin + r4);
                const char* p = (const char*)wmod + (size_t)r4 * 6144 + (xcd * 4 + c) * 64 + wofs;
#pragma unroll
                for (int i = 0; i < 4; ++i) rm[i] = ldnt(p + i * 6144);
            }
            const v4u xa = *(const GAS v4u*)(xin + 8 * l);
            const v2u xo = *(const GAS v2u*)(xin + r4);
            {
                const char* p = (const char*)wq + (size_t)r4 * 8192 + (g * 512 + c * 32) * 2 + wofs;
#pragma unroll
                for (int i = 0; i < 4; ++i) rq[i] = ldnt(p + i * 8192);
            }
            __builtin_amdgcn_sched_barrier(0);
            v2u op[8];
            if (ismod) {
                v2u a = {pk2(silu_bf(lo16(cv.x)), silu_bf(hi16(cv.x))), pk2(silu_bf(lo16(cv.y)), silu_bf(hi16(cv.y)))};
                perm4(op, rm);
                gemv32(red0, a, op, w, l);
                TS(1);
                bar();
            }
            float xv[8] = {lo16(xa.x), hi16(xa.x), lo16(xa.y), hi16(xa.y), lo16(xa.z), hi16(xa.z), lo16(xa.w), hi16(xa.w)};
            float s = ((xv[0] + xv[1]) + (xv[2] + xv[3])) + ((xv[4] + xv[5]) + (xv[6] + xv[7]));
            s += dpp<QP1>(s); s += dpp<QP2>(s); s += dpp<ROR(4)>(s); s += dpp<ROR(8)>(s); s = x32add(x16add(s));
            const float mean = s * (1.f / 512.f);
            float v = 0.f;
#pragma unroll
            for (int i = 0; i < 8; ++i) { float d = xv[i] - mean; v += d * d; }
            v += dpp<QP1>(v); v += dpp<QP2>(v); v += dpp<ROR(4)>(v); v += dpp<ROR(8)>(v); v = x32add(x16add(v));
            const float mb = rbf(mean);
            const float dn = rbf(__builtin_sqrtf(rbf(rbf(v * (1.f / 512.f)) + 1e-5f)));
            float n[4] = {rbf(rbf(lo16(xo.x) - mb) / dn), rbf(rbf(hi16(xo.x) - mb) / dn), rbf(rbf(lo16(xo.y) - mb) / dn), rbf(rbf(hi16(xo.y) - mb) / dn)};
            perm4(op, rq);
            const u32 tag = epoch * 1664525u + 1013904223u;
            TS(2);
            {
                v4u v2[2]; const char* const pp[2] = {ws + OFF_XH + r4 * 4, ws + OFF_XH + 2048 + r4 * 4};
                gather<2>(v2, pp, tag);
                TS(3);
                v2u a;
                a.x = pk2(rbf(n[0] * rbf(1.f + lo16(v2[1].x))) + lo16(v2[0].x), rbf(n[1] * rbf(1.f + hi16(v2[1].x))) + hi16(v2[0].x));
                a.y = pk2(rbf(n[2] * rbf(1.f + lo16(v2[1].z))) + lo16(v2[0].z), rbf(n[3] * rbf(1.f + hi16(v2[1].z))) + hi16(v2[0].z));
                gemv32(red1, a, op, w, l);
            }
            bar();
            TS(4);
            if (DLY_WO) __builtin_amdgcn_s_sleep(DLY_WO);
            {
                const char* p = (const char*)wo + (size_t)(g * 512 + 8 * (l >> 2) + w) * 1024 + c * 64 + wofs;
#pragma unroll
                for (int i = 0; i < 4; ++i) rq[i] = ldnt(p + i * 131072);
            }
            __builtin_amdgcn_sched_barrier(0);
            bar();
            TS(5);
            v4f wv[4];
#pragma unroll
            for (int i = 0; i < 4; ++i) wv[i] = *(const v4f*)(wt + (4 * (l & 3) + i) * 4);
            perm4(op, rq);
            TS(6);
            {
                v4u va[4];
                const char* base = ws + OFF_XA + (g * 16 + 4 * (l & 3)) * 2048 + (16 * w + (l >> 2)) * 16;
                const char* const pp[4] = {base, base + 2048, base + 4096, base + 6144};
                gather<4>(va, pp, tag);
                TS(7);
                v2f a01 = {0.f, 0.f}, a23 = {0.f, 0.f};
#pragma unroll
                for (int i = 0; i < 4; ++i) {
                    a01 += v2f{asf(va[i].x), lo16(va[i].x)} * v2f{wv[i][0], wv[i][1]};
                    a23 += v2f{asf(va[i].z), lo16(va[i].z)} * v2f{wv[i][2], wv[i][3]};
                }
                float o1 = a01[0], o0 = a01[1], o3 = a23[0], o2 = a23[1];
                o0 += dpp<QP1>(o0); o1 += dpp<QP1>(o1); o2 += dpp<QP1>(o2); o3 += dpp<QP1>(o3);
                o0 += dpp<QP2>(o0); o1 += dpp<QP2>(o1); o2 += dpp<QP2>(o2); o3 += dpp<QP2>(o3);
                gemv32(red2, v2u{pk2(o0, o1), pk2(o2, o3)}, op, w, l);
            }
            bar();
            TS(8);
        } else {
            const u32 tag = epoch * 1664525u + 1013904223u;
            if (c < 4) { bar(); pub_bf16(ws + OFF_XH + (xcd * 4 + c) * 128, red32(red0, l), tag, l); TS(1); }
            bar();
            pub_bf16(ws + OFF_XQ + (g * 16 + c) * 128, red32(red1, l), tag, l);
            TS(4);
            {
                v4u vs[1]; const char* const pp[1] = {ws + OFF_XS + (g * 16 + (l & 15)) * 128 + ((l >> 4) & 1) * 16};
                gather<1>(vs, pp, tag);
                TS(5);
                float z0 = asf(vs[0].x), z1 = asf(vs[0].z), m0 = z0, m1 = z1;
                m0 = fmaxf(m0, dpp<QP1>(m0)); m1 = fmaxf(m1, dpp<QP1>(m1)); m0 = fmaxf(m0, dpp<QP2>(m0)); m1 = fmaxf(m1, dpp<QP2>(m1));
                m0 = fmaxf(m0, dpp<ROR(4)>(m0)); m1 = fmaxf(m1, dpp<ROR(4)>(m1)); m0 = fmaxf(m0, dpp<ROR(8)>(m0)); m1 = fmaxf(m1, dpp<ROR(8)>(m1));
                float e0 = __builtin_amdgcn_exp2f(z0 - m0), e1 = __builtin_amdgcn_exp2f(z1 - m1), s0 = e0, s1 = e1;
                s0 += dpp<QP1>(s0); s1 += dpp<QP1>(s1); s0 += dpp<QP2>(s0); s1 += dpp<QP2>(s1);
                s0 += dpp<ROR(4)>(s0); s1 += dpp<ROR(4)>(s1); s0 += dpp<ROR(8)>(s0); s1 += dpp<ROR(8)>(s1);
                if (l < 32) *(v2f*)(wt + (l & 15) * 4 + (l >> 4) * 2) = v2f{e1 / s1, e0 / s0};
            }
            bar();
            bar();
            {
                float r = red32(red2, l), r1 = dpp<QP1>(r);
                if (l < 32 && (l & 1) == 0) st16(ws + OFF_XW + (c * 8 + g) * 256 + l * 8, v4u{asu(r), tag, asu(r1), tag});
            }
            TS(8);
            if (c < 4) {
                const int col = (xcd * 4 + c) * 16 + 2 * (l & 7), pg = l >> 3;
                const u32 xr = *(const GAS u32*)(xin + col);
                v4u v3[3];
                const char* const pp[3] = {ws + OFF_XW + ((col >> 5) * 8 + pg) * 256 + (col & 31) * 8, ws + OFF_XM + (col & ~3) * 4, ws + OFF_XM + 6144 + (col & ~3) * 4};
                gather<3>(v3, pp, tag);
                float a0 = asf(v3[0].x), a1 = asf(v3[0].z);
                a0 += dpp<ROR(8)>(a0); a1 += dpp<ROR(8)>(a1); a0 = x32add(x16add(a0)); a1 = x32add(x16add(a1));
                const u32 ga = (l & 1) ? v3[1].z : v3[1].x, gb = (l & 1) ? v3[2].z : v3[2].x;
                const float x20 = rbf(lo16(xr) + rbf(lo16(ga) * rbf(a0))), x21 = rbf(hi16(xr) + rbf(hi16(ga) * rbf(a1)));
                TS(9);
                v4u vd[7];
                const char* base = ws + OFF_XD + pg * 4096 + col * 8;
                const char* const pd[7] = {base, base + 32768, base + 65536, base + 98304, base + 131072, base + 163840, base + 196608};
                gather<7>(vd, pd, tag);
                TS(10);
                float y0 = 0.f, y1 = 0.f;
#pragma unroll
                for (int i = 0; i < 7; ++i) { y0 += asf(vd[i].x); y1 += asf(vd[i].z); }
                y0 += dpp<ROR(8)>(y0); y1 += dpp<ROR(8)>(y1); y0 = x32add(x16add(y0)); y1 = x32add(x16add(y1));
                const u32 o = pk2(x20 + rbf(lo16(gb) * rbf(y0)), x21 + rbf(hi16(gb) * rbf(y1)));
                if (l < 8) *(GAS u32*)(out + col) = o;
                if (b == 128 && l == 0) __hip_atomic_store((GAS u32*)(ws + OFF_EP), tag, __ATOMIC_RELAXED, __HIP_MEMORY_SCOPE_AGENT);
                TS(11);
            }
        }
    } else {
        const int g = xcd, s = idx;
        const bool isM = idx < 7, isR = idx >= 7 && idx < 15;
        if (w < 8) {
            if (DLY_A) __builtin_amdgcn_s_sleep(DLY_A);
            v4u rk[8], rv[8];
            const size_t kb = ((size_t)g * 65536 + 61440 + 256 * s + 32 * w) * 256;
            {
                const char* p = (const char*)kc + kb + (l & 15) * 256 + (l >> 4) * 16;
#pragma unroll
                for (int i = 0; i < 8; ++i) rk[i] = ldnt(p + (i >> 2) * 4096 + (i & 3) * 64);
                const char* q = (const char*)vc + kb + (l >> 4) * 1024 + (l & 15) * 16;
#pragma unroll
                for (int i = 0; i < 8; ++i) rv[i] = ldnt(q + (i >> 2) * 4096 + (i & 3) * 256);
            }
            __builtin_amdgcn_sched_barrier(0);
            TS(1);
            bar();
            TS(2);
            v4f sa = {0.f, 0.f, 0.f, 0.f}, sb = sa;
#pragma unroll
            for (int t = 0; t < 4; ++t) {
                const v4u qv = *(const v4u*)(qs + (l & 3) * 64 + (4 * t + (l >> 4)) * 4);
                sa = mfma32(rk[t], qv, sa); sb = mfma32(rk[4 + t], qv, sb);
            }
            float e[8], mw;
#pragma unroll
            for (int i = 0; i < 4; ++i) { e[i] = rbf(rbf(sa[i]) * 0.08838834764831845f); e[4 + i] = rbf(rbf(sb[i]) * 0.08838834764831845f); }
            mw = fmaxf(fmaxf(fmaxf(e[0], e[1]), fmaxf(e[2], e[3])), fmaxf(fmaxf(e[4], e[5]), fmaxf(e[6], e[7])));
            mw = x32max(x16max(mw));
            float lw = 0.f;
#pragma unroll
            for (int i = 0; i < 8; ++i) { e[i] = __builtin_amdgcn_exp2f((e[i] - mw) * 1.4426950408889634f); lw += e[i]; }
            lw = x32add(x16add(lw));
            if (l < 4) *(v2f*)(st + l * 16 + w * 2) = v2f{mw, lw};
            TS(3);
            bar();
            v4u pa; float linv;
            {
                const v4f* q = (const v4f*)(st + (l & 3) * 16);
                const v4f s0 = q[0], s1 = q[1], s2 = q[2], s3 = q[3];
                const float mc = fmaxf(fmaxf(fmaxf(s0[0], s0[2]), fmaxf(s1[0], s1[2])), fmaxf(fmaxf(s2[0], s2[2]), fmaxf(s3[0], s3[2])));
                const float f = __builtin_amdgcn_exp2f((mw - mc) * 1.4426950408889634f);
                pa = v4u{pk2(e[0] * f, e[1] * f), pk2(e[2] * f, e[3] * f), pk2(e[4] * f, e[5] * f), pk2(e[6] * f, e[7] * f)};
                float lc = 0.f;
                lc += s0[1] * __builtin_amdgcn_exp2f((s0[0] - mc) * 1.4426950408889634f); lc += s0[3] * __builtin_amdgcn_exp2f((s0[2] - mc) * 1.4426950408889634f);
                lc += s1[1] * __builtin_amdgcn_exp2f((s1[0] - mc) * 1.4426950408889634f); lc += s1[3] * __builtin_amdgcn_exp2f((s1[2] - mc) * 1.4426950408889634f);
                lc += s2[1] * __builtin_amdgcn_exp2f((s2[0] - mc) * 1.4426950408889634f); lc += s2[3] * __builtin_amdgcn_exp2f((s2[2] - mc) * 1.4426950408889634f);
                lc += s3[1] * __builtin_amdgcn_exp2f((s3[0] - mc) * 1.4426950408889634f); lc += s3[3] * __builtin_amdgcn_exp2f((s3[2] - mc) * 1.4426950408889634f);
                linv = 1.f / lc;
            }
            const float li0 = dpp<0x00>(linv), li1 = dpp<0x55>(linv), li2 = dpp<0xAA>(linv), li3 = dpp<0xFF>(linv);
            TS(4);
            v4u vo[8];
#pragma unroll
            for (int pr = 0; pr < 4; ++pr)
#pragma unroll
                for (int j = 0; j < 8; ++j) vo[j][pr] = __builtin_amdgcn_perm(rv[2 * pr + 1][j >> 1], rv[2 * pr][j >> 1], (j & 1) ? 0x07060302u : 0x05040100u);
            TS(5);
            v4u r0[4], r1[4], r2[4]; v2u cv;
            const int jm = xcd * 7 + idx;
            if (isM) {
                const size_t o = (size_t)r4 * 3584 + jm * 64 + wofs;
#pragma unroll
                for (int i = 0; i < 4; ++i) r0[i] = ldnt((const char*)wg + o + i * 3584);
#pragma unroll
                for (int i = 0; i < 4; ++i) r1[i] = ldnt((const char*)wu + o + i * 3584);
                const char* p = (const char*)wd + (size_t)(32 * jm + 4 * (l >> 3)) * 1024 + 128 * w + (l & 7) * 16;
#pragma unroll
                for (int i = 0; i < 4; ++i) r2[i] = ldnt(p + i * 1024);
                cv = *(const GAS v2u*)(xin + r4);
            } else if (isR) {
                const char* p = (const char*)wmod + (size_t)r4 * 6144 + 2048 + (xcd * 8 + idx - 7) * 64 + wofs;
#pragma unroll
                for (int i = 0; i < 4; ++i) r0[i] = ldnt(p + i * 6144);
                cv = *(const GAS v2u*)(cin + r4);
            }
            __builtin_amdgcn_sched_barrier(0);
            {
                v4f d[8];
#pragma unroll
                for (int j = 0; j < 8; ++j) d[j] = mfma32(pa, vo[j], v4f{0.f, 0.f, 0.f, 0.f});
                if (l < 16) {
#pragma unroll
                    for (int j = 0; j < 8; ++j) po[(w * 8 + j) * 16 + l] = d[j];
                }
            }
            bar();
            const u32 tag = epoch * 1664525u + 1013904223u;
            if (w < 2) {
                v4f o = po[tid];
#pragma unroll
                for (int i = 1; i < 8; ++i) o += po[i * 128 + tid];
                const u32 l0 = pk2(o[0] * li0, o[2] * li2);
                const u32 b1 = asu(o[1] * li1), b3 = asu(o[3] * li3);
                const u32 q0 = l0 & 0xffffu, q2 = l0 >> 16;
                const u32 m1 = (b1 & 0x7fffffffu) + 0x8000u, m3 = (b3 & 0x7fffffffu) + 0x8000u;
                const u32 h1 = (m1 > q0 ? m1 - q0 : 0u) & 0x7fff0000u, h3 = (m3 > q2 ? m3 - q2 : 0u) & 0x7fff0000u;
                st16(ws + OFF_XA + (g * 16 + s) * 2048 + tid * 16, v4u{(b1 & 0x80000000u) | h1 | q0, tag, (b3 & 0x80000000u) | h3 | q2, tag});
            }
            TS(6);
            if (isR) {
                v2u op[8];
                v2u a = {pk2(silu_bf(lo16(cv.x)), silu_bf(hi16(cv.x))), pk2(silu_bf(lo16(cv.y)), silu_bf(hi16(cv.y)))};
                perm4(op, r0);
                gemv32(red0, a, op, w, l);
                bar();
                TS(7);
            } else if (isM) {
                v2u og[8], ou[8], od[8];
                perm4(og, r0); perm4(ou, r1); perm4(od, r2);
                TS(7);
                v4u vm[3];
                { const char* base = ws + OFF_XM + r4 * 4; const char* const pp[3] = {base, base + 2048, base + 4096}; gather<3>(vm, pp, tag); }
                TS(8);
                float x2[4];
                {
                    v4u vw[4];
                    const char* base = ws + OFF_XW + ((r4 >> 5) * 8 + 2 * (l & 3)) * 256 + (r4 & 31) * 8;
                    const char* const pp[4] = {base, base + 16, base + 256, base + 272};
                    gather<4>(vw, pp, tag);
                    TS(9);
                    float a0 = asf(vw[0].x) + asf(vw[2].x), a1 = asf(vw[0].z) + asf(vw[2].z), a2 = asf(vw[1].x) + asf(vw[3].x), a3 = asf(vw[1].z) + asf(vw[3].z);
                    a0 += dpp<QP1>(a0); a1 += dpp<QP1>(a1); a2 += dpp<QP1>(a2); a3 += dpp<QP1>(a3);
                    a0 += dpp<QP2>(a0); a1 += dpp<QP2>(a1); a2 += dpp<QP2>(a2); a3 += dpp<QP2>(a3);
                    x2[0] = rbf(lo16(cv.x) + rbf(lo16(vm[0].x) * rbf(a0))); x2[1] = rbf(hi16(cv.x) + rbf(hi16(vm[0].x) * rbf(a1)));
                    x2[2] = rbf(lo16(cv.y) + rbf(lo16(vm[0].z) * rbf(a2))); x2[3] = rbf(hi16(cv.y) + rbf(hi16(vm[0].z) * rbf(a3)));
                }
                {
                    const float pv = asf(__builtin_amdgcn_readfirstlane(asu(x2[0])));
                    const float d0 = x2[0] - pv, d1 = x2[1] - pv, d2 = x2[2] - pv, d3 = x2[3] - pv;
                    float s1 = (d0 + d1) + (d2 + d3), s2 = (d0 * d0 + d1 * d1) + (d2 * d2 + d3 * d3);
                    s1 += dpp<ROR(4)>(s1); s2 += dpp<ROR(4)>(s2); s1 += dpp<ROR(8)>(s1); s2 += dpp<ROR(8)>(s2);
                    s1 = x32add(x16add(s1)); s2 = x32add(x16add(s2));
                    if (l == 0) *(v4f*)(lns + w * 4) = v4f{pv, s1, s2, 0.f};
                }
                bar();
                {
                    const v4f t = *(const v4f*)(lns + (l & 7) * 4);
                    float a = 64.f * t[0] + t[1];
                    a += dpp<QP1>(a); a += dpp<QP2>(a); a += dpp<ROR(4)>(a) ;
                    const float mean = a * (1.f / 512.f), ee = mean - t[0];
                    float v = t[2] - 2.f * ee * t[1] + 64.f * ee * ee;
                    v += dpp<QP1>(v); v += dpp<QP2>(v); v += dpp<ROR(4)>(v);
                    const float mb = rbf(mean);
                    const float dn = rbf(__builtin_sqrtf(rbf(rbf(fmaxf(v, 0.f) * (1.f / 512.f)) + 1e-5f)));
                    const float ri = 1.f / dn;
                    v2u a2;
                    a2.x = pk2(rbf(rbf(rbf(x2[0] - mb) * ri) * rbf(1.f + lo16(vm[2].x))) + lo16(vm[1].x), rbf(rbf(rbf(x2[1] - mb) * ri) * rbf(1.f + hi16(vm[2].x))) + hi16(vm[1].x));
                    a2.y = pk2(rbf(rbf(rbf(x2[2] - mb) * ri) * rbf(1.f + lo16(vm[2].z))) + lo16(vm[1].z), rbf(rbf(rbf(x2[3] - mb) * ri) * rbf(1.f + hi16(vm[2].z))) + hi16(vm[1].z));
                    gemv32(red0, a2, og, w, l);
                    gemv32(red1, a2, ou, w, l);
                }
                bar();
                TS(10);
                {
                    const float* rr = (l < 32) ? red0 : red1;
                    const v4f t = *(const v4f*)(rr + (4 * w + ((l >> 3) & 3)) * 36 + (l & 7) * 4);
                    float a = (t[0] + t[1]) + (t[2] + t[3]);
                    a += dpp<QP1>(a); a += dpp<QP2>(a); a += dpp<0x141>(a);
                    auto r = __builtin_amdgcn_permlane32_swap(asu(a), asu(a), false, false);
                    const float gg = rbf(asf(r[0])), uu = rbf(asf(r[1]));
                    const float tt = 0.7978845608028654f * (gg + 0.044715f * gg * gg * gg);
                    const float th = 1.f - 2.f * __builtin_amdgcn_rcpf(1.f + __builtin_amdgcn_exp2f(tt * 2.885390081777927f));
                    const float hv = rbf(0.5f * gg * (1.f + th)) * uu;
                    const u32 hp = pk2(hv, dpp<ROR(8)>(hv));
                    if ((l & 47) == 0) hhs[2 * w + (l >> 4)] = hp;
                }
                bar();
                {
                    const v2u a = *(const v2u*)(hhs + 2 * (2 * (l >> 4) + (l & 1)));
                    float y[8];
#pragma unroll
                    for (int j = 0; j < 8; ++j) { const v4f d = mfma16(a, od[j]); y[j] = d[0] + dpp<ROR(8)>(d[1]); }
                    const int q = l >> 4;
                    const float y0 = q == 0 ? y[0] : q == 1 ? y[2] : q == 2 ? y[4] : y[6];
                    const float y1 = q == 0 ? y[1] : q == 1 ? y[3] : q == 2 ? y[5] : y[7];
                    if ((l & 8) == 0) st16(ws + OFF_XD + jm * 4096 + (64 * w + 8 * (l & 7) + 2 * q) * 8, v4u{asu(y0), tag, asu(y1), tag});
                }
                TS(11);
            }
        } else {
            const u32 tag = epoch * 1664525u + 1013904223u;
            {
                v4u v2[2]; const char* base = ws + OFF_XQ + g * 2048 + l * 16; const char* const pp[2] = {base, base + 1024};
                gather<2>(v2, pp, tag);
                *(v2u*)(qs + 2 * l) = v2u{v2[0].x, v2[0].z};
                *(v2u*)(qs + 128 + 2 * l) = v2u{v2[1].x, v2[1].z};
            }
            TS(2);
            bar();
            bar();
            {
                const v4f* q = (const v4f*)(st + (l & 3) * 16);
                const v4f s0 = q[0], s1 = q[1], s2 = q[2], s3 = q[3];
                const float mc = fmaxf(fmaxf(fmaxf(s0[0], s0[2]), fmaxf(s1[0], s1[2])), fmaxf(fmaxf(s2[0], s2[2]), fmaxf(s3[0], s3[2])));
                float lc = 0.f;
                lc += s0[1] * __builtin_amdgcn_exp2f((s0[0] - mc) * 1.4426950408889634f); lc += s0[3] * __builtin_amdgcn_exp2f((s0[2] - mc) * 1.4426950408889634f);
                lc += s1[1] * __builtin_amdgcn_exp2f((s1[0] - mc) * 1.4426950408889634f); lc += s1[3] * __builtin_amdgcn_exp2f((s1[2] - mc) * 1.4426950408889634f);
                lc += s2[1] * __builtin_amdgcn_exp2f((s2[0] - mc) * 1.4426950408889634f); lc += s2[3] * __builtin_amdgcn_exp2f((s2[2] - mc) * 1.4426950408889634f);
                lc += s3[1] * __builtin_amdgcn_exp2f((s3[0] - mc) * 1.4426950408889634f); lc += s3[3] * __builtin_amdgcn_exp2f((s3[2] - mc) * 1.4426950408889634f);
                const float z = mc * 1.4426950408889634f + __builtin_amdgcn_logf(lc);
                const float z1 = dpp<QP1>(z);
                if (l == 0 || l == 2) st16(ws + OFF_XS + (g * 16 + s) * 128 + l * 8, v4u{asu(z), tag, asu(z1), tag});
            }
            TS(4);
            bar();
            if (isR) { bar(); pub_bf16(ws + OFF_XM + (xcd * 8 + idx - 7) * 128, red32(red0, l), tag, l); TS(7); }
            else if (isM) { bar(); bar(); bar(); }
        }
    }
    TSDUMP();
}

extern "C" void kernel_launch(void* const* d_in, const int* in_sizes, int n_in, void* d_out, int out_size, void* d_ws, size_t ws_size, hipStream_t stream) {
    fused_kernel<<<256, 576, 0, stream>>>((const u16*)d_in[0], (const u16*)d_in[1], (const u16*)d_in[2], (const u16*)d_in[3], (const u16*)d_in[4],
        (const u16*)d_in[6], (const u16*)d_in[7], (const u16*)d_in[8], (const u16*)d_in[9], (const u16*)d_in[10], (u16*)d_out, (char*)d_ws);
}
```

```cpp
#include <hip/hip_runtime.h>
#include <stdint.h>

typedef unsigned u32;
typedef unsigned short u16;
typedef u32 v4u __attribute__((ext_vector_type(4)));
typedef u32 v2u __attribute__((ext_vector_type(2)));
typedef float v4f __attribute__((ext_vector_type(4)));
typedef float v2f __attribute__((ext_vector_type(2)));
typedef short v4s __attribute__((ext_vector_type(4)));
typedef __bf16 v8bf __attribute__((ext_vector_type(8)));
typedef __bf16 v2bf __attribute__((ext_vector_type(2)));
#define GAS __attribute__((address_space(1)))

#define PF_N 8
#define PF_L 32
#define DLY_A 0
#define DLY_WO 0

constexpr u32 OFF_EP = 0;
constexpr u32 OFF_XH = 4096;
constexpr u32 OFF_XM = 8192;
constexpr u32 OFF_XQ = 16384;
constexpr u32 OFF_XS = 32768;
constexpr u32 OFF_XW = 49152;
constexpr u32 OFF_XA = 131072;
constexpr u32 OFF_XD = 393216;
constexpr u32 OFF_DBG = 1u << 20;
constexpr int SPIN_MAX = 1 << 16;

__device__ __forceinline__ float asf(u32 v) { return __uint_as_float(v); }
__device__ __forceinline__ u32 asu(float v) { return __float_as_uint(v); }
__device__ __forceinline__ float lo16(u32 v) { return asf(v << 16); }
__device__ __forceinline__ float hi16(u32 v) { return asf(v & 0xffff0000u); }
__device__ __forceinline__ u32 pk2(float a, float b) { v2f t = {a, b}; v2bf r = __builtin_convertvector(t, v2bf); return __builtin_bit_cast(u32, r); }
__device__ __forceinline__ float rbf(float a) { return lo16(pk2(a, a) & 0xffffu); }
__device__ __forceinline__ v4u ldnt(const void* p) { return __builtin_nontemporal_load((const GAS v4u*)p); }
__device__ __forceinline__ void st16(void* p, v4u v) { asm volatile("global_store_dwordx4 %0, %1, off sc1\n s_nop 1" : : "v"(p), "v"(v) : "memory"); }
__device__ __forceinline__ v4u ld16(const void* p) { v4u v; asm volatile("global_load_dwordx4 %0, %1, off sc1" : "=v"(v) : "v"(p) : "memory"); return v; }
__device__ __forceinline__ void bar() { asm volatile("s_waitcnt lgkmcnt(0)\n s_barrier" ::: "memory"); }
template <int C> __device__ __forceinline__ float dpp(float x) { return asf(__builtin_amdgcn_update_dpp(0, asu(x), C, 0xf, 0xf, true)); }
#define QP1 0xB1
#define QP2 0x4E
#define ROR(n) (0x120 + (n))
__device__ __forceinline__ float x16add(float x) { auto r = __builtin_amdgcn_permlane16_swap(asu(x), asu(x), false, false); return asf(r[0]) + asf(r[1]); }
__device__ __forceinline__ float x32add(float x) { auto r = __builtin_amdgcn_permlane32_swap(asu(x), asu(x), false, false); return asf(r[0]) + asf(r[1]); }
__device__ __forceinline__ float x16max(float x) { auto r = __builtin_amdgcn_permlane16_swap(asu(x), asu(x), false, false); return fmaxf(asf(r[0]), asf(r[1])); }
__device__ __forceinline__ float x32max(float x) { auto r = __builtin_amdgcn_permlane32_swap(asu(x), asu(x), false, false); return fmaxf(asf(r[0]), asf(r[1])); }

template <int N> __device__ __forceinline__ void gather(v4u (&v)[N], const char* const (&p)[N], u32 tag) {
    for (int spin = 0; spin < SPIN_MAX; ++spin) {
#pragma unroll
        for (int i = 0; i < N; ++i) v[i] = ld16(p[i]);
        asm volatile("s_waitcnt vmcnt(0)" ::: "memory");
        u32 bad = 0;
#pragma unroll
        for (int i = 0; i < N; ++i) { asm volatile("" : "+v"(v[i])); bad |= (v[i].y ^ tag) | (v[i].w ^ tag); }
        if (__all(bad == 0)) break;
    }
}
__device__ __forceinline__ void perm4(v2u (&op)[8], const v4u (&r)[4]) {
#pragma unroll
    for (int j = 0; j < 8; ++j) {
        const u32 sel = (j & 1) ? 0x07060302u : 0x05040100u;
        op[j].x = __builtin_amdgcn_perm(r[1][j >> 1], r[0][j >> 1], sel);
        op[j].y = __builtin_amdgcn_perm(r[3][j >> 1], r[2][j >> 1], sel);
    }
}
__device__ __forceinline__ v4f mfma16(v2u a, v2u b) {
    return __builtin_amdgcn_mfma_f32_16x16x16bf16_1k(__builtin_bit_cast(v4s, a), __builtin_bit_cast(v4s, b), v4f{0.f, 0.f, 0.f, 0.f}, 0, 0, 0);
}
__device__ __forceinline__ v4f mfma32(v4u a, v4u b, v4f c) {
    return __builtin_amdgcn_mfma_f32_16x16x32_bf16(__builtin_bit_cast(v8bf, a), __builtin_bit_cast(v8bf, b), c, 0, 0, 0);
}
__device__ __forceinline__ void gemv32(float* red, v2u a, const v2u (&op)[8], int w, int l) {
    v4f d[8];
#pragma unroll
    for (int j = 0; j < 8; ++j) d[j] = mfma16(a, op[j]);
    if (((l >> 2) & 3) == (l >> 4)) {
        float* q = red + (8 * (l & 3)) * 36 + 4 * w + (l >> 4);
#pragma unroll
        for (int j = 0; j < 8; ++j) q[j * 36] = d[j][0];
    }
}
__device__ __forceinline__ float red32(const float* red, int l) {
    const v4f* q = (const v4f*)(red + (l & 31) * 36 + 16 * (l >> 5));
    v4f s = (q[0] + q[1]) + (q[2] + q[3]);
    return x32add((s[0] + s[1]) + (s[2] + s[3]));
}
__device__ __forceinline__ void pub_bf16(char* line, float r, u32 tag, int l) {
    float r1 = dpp<QP1>(r);
    u32 p = pk2(r, r1);
    u32 p2 = __builtin_amdgcn_update_dpp(0, p, QP2, 0xf, 0xf, true);
    if (l < 32 && (l & 3) == 0) st16(line + l * 4, v4u{p, tag, p2, tag});
}
__device__ __forceinline__ float silu_bf(float v) { return v * __builtin_amdgcn_rcpf(1.f + __expf(-v)); }

#define TS(i)
#define TSDUMP()

__global__ void __launch_bounds__(576) fused_kernel(const u16* __restrict__ xin, const u16* __restrict__ kc, const u16* __restrict__ vc,
        const u16* __restrict__ wq, const u16* __restrict__ wo, const u16* __restrict__ wg, const u16* __restrict__ wu,
        const u16* __restrict__ wd, const u16* __restrict__ cin, const u16* __restrict__ wmod, u16* __restrict__ out, char* __restrict__ ws) {
    __shared__ __attribute__((aligned(16))) float red0[32 * 36];
    __shared__ __attribute__((aligned(16))) float red1[32 * 36];
    __shared__ __attribute__((aligned(16))) float red2[32 * 36];
    __shared__ __attribute__((aligned(16))) v4f po[8 * 128];
    __shared__ __attribute__((aligned(16))) u32 qs[256];
    __shared__ __attribute__((aligned(16))) float st[64];
    __shared__ __attribute__((aligned(16))) float wt[64];
    __shared__ __attribute__((aligned(16))) float lns[32];
    __shared__ __attribute__((aligned(16))) u32 hhs[16];
    const int b = blockIdx.x, tid = threadIdx.x, l = tid & 63;
    const int w = __builtin_amdgcn_readfirstlane(tid >> 6);
    const int xcd = b & 7, idx = b >> 3;
    TS(0);

    u32 pfd = 0;
    if (w == 8) {
        uint64_t pc; u32 len;
        asm volatile("s_getpc_b64 %0" : "=s"(pc)); len = 15000;
        const u32 off = (u32)((idx & (PF_N - 1)) * PF_L + (l & (PF_L - 1))) * 128u;
        if (l < PF_L && off < len) asm volatile("global_load_dword %0, %1, off" : "+v"(pfd) : "v"(pc + off) : "memory");
    }
    const u32 epoch = __hip_atomic_load((const GAS u32*)(ws + OFF_EP), __ATOMIC_RELAXED, __HIP_MEMORY_SCOPE_AGENT);
    const int r4 = 64 * w + (l & 60);
    const u32 wofs = (u32)(l & 3) * 16;

    if (idx >= 16) {
        const int c = idx - 16, g = (xcd + 1) & 7;
        if (w < 8) {
            v4u rm[4], rq[4]; v2u cv;
            const bool ismod = c < 4;
            if (ismod) {
                cv = *(const GAS v2u*)(cin + r4);
                const char* p = (const char*)wmod + (size_t)r4 * 6144 + (xcd * 4 + c) * 64 + wofs;
#pragma unroll
                for (int i = 0; i < 4; ++i) rm[i] = ldnt(p + i * 6144);
            }
            const v4u xa = *(const GAS v4u*)(xin + 8 * l);
            const v2u xo = *(const GAS v2u*)(xin + r4);
            {
                const char* p = (const char*)wq + (size_t)r4 * 8192 + (g * 512 + c * 32) * 2 + wofs;
#pragma unroll
                for (int i = 0; i < 4; ++i) rq[i] = ldnt(p + i * 8192);
            }
            __builtin_amdgcn_sched_barrier(0);
            v2u op[8];
            if (ismod) {
                v2u a = {pk2(silu_bf(lo16(cv.x)), silu_bf(hi16(cv.x))), pk2(silu_bf(lo16(cv.y)), silu_bf(hi16(cv.y)))};
                perm4(op, rm);
                gemv32(red0, a, op, w, l);
                TS(1);
                bar();
            }
            float xv[8] = {lo16(xa.x), hi16(xa.x), lo16(xa.y), hi16(xa.y), lo16(xa.z), hi16(xa.z), lo16(xa.w), hi16(xa.w)};
            float s = ((xv[0] + xv[1]) + (xv[2] + xv[3])) + ((xv[4] + xv[5]) + (xv[6] + xv[7]));
            s += dpp<QP1>(s); s += dpp<QP2>(s); s += dpp<ROR(4)>(s); s += dpp<ROR(8)>(s); s = x32add(x16add(s));
            const float mean = s * (1.f / 512.f);
            float v = 0.f;
#pragma unroll
            for (int i = 0; i < 8; ++i) { float d = xv[i] - mean; v += d * d; }
            v += dpp<QP1>(v); v += dpp<QP2>(v); v += dpp<ROR(4)>(v); v += dpp<ROR(8)>(v); v = x32add(x16add(v));
            const float mb = rbf(mean);
            const float dn = rbf(__builtin_sqrtf(rbf(rbf(v * (1.f / 512.f)) + 1e-5f)));
            float n[4] = {rbf(rbf(lo16(xo.x) - mb) / dn), rbf(rbf(hi16(xo.x) - mb) / dn), rbf(rbf(lo16(xo.y) - mb) / dn), rbf(rbf(hi16(xo.y) - mb) / dn)};
            perm4(op, rq);
            const u32 tag = epoch * 1664525u + 1013904223u;
            TS(2);
            {
                v4u v2[2]; const char* const pp[2] = {ws + OFF_XH + r4 * 4, ws + OFF_XH + 2048 + r4 * 4};
                gather<2>(v2, pp, tag);
                TS(3);
                v2u a;
                a.x = pk2(rbf(n[0] * rbf(1.f + lo16(v2[1].x))) + lo16(v2[0].x), rbf(n[1] * rbf(1.f + hi16(v2[1].x))) + hi16(v2[0].x));
                a.y = pk2(rbf(n[2] * rbf(1.f + lo16(v2[1].z))) + lo16(v2[0].z), rbf(n[3] * rbf(1.f + hi16(v2[1].z))) + hi16(v2[0].z));
                gemv32(red1, a, op, w, l);
            }
            bar();
            TS(4);
            if (DLY_WO) __builtin_amdgcn_s_sleep(DLY_WO);
            {
                const char* p = (const char*)wo + (size_t)(g * 512 + 8 * (l >> 2) + w) * 1024 + c * 64 + wofs;
#pragma unroll
                for (int i = 0; i < 4; ++i) rq[i] = ldnt(p + i * 131072);
            }
            __builtin_amdgcn_sched_barrier(0);
            bar();
            TS(5);
            v4f wv[4];
#pragma unroll
            for (int i = 0; i < 4; ++i) wv[i] = *(const v4f*)(wt + (4 * (l & 3) + i) * 4);
            perm4(op, rq);
            TS(6);
            {
                v4u va[4];
                const char* base = ws + OFF_XA + (g * 16 + 4 * (l & 3)) * 2048 + (16 * w + (l >> 2)) * 16;
                const char* const pp[4] = {base, base + 2048, base + 4096, base + 6144};
                gather<4>(va, pp, tag);
                TS(7);
                v2f a01 = {0.f, 0.f}, a23 = {0.f, 0.f};
#pragma unroll
                for (int i = 0; i < 4; ++i) {
                    a01 += v2f{asf(va[i].x), lo16(va[i].x)} * v2f{wv[i][0], wv[i][1]};
                    a23 += v2f{asf(va[i].z), lo16(va[i].z)} * v2f{wv[i][2], wv[i][3]};
                }
                float o1 = a01[0], o0 = a01[1], o3 = a23[0], o2 = a23[1];
                o0 += dpp<QP1>(o0); o1 += dpp<QP1>(o1); o2 += dpp<QP1>(o2); o3 += dpp<QP1>(o3);
                o0 += dpp<QP2>(o0); o1 += dpp<QP2>(o1); o2 += dpp<QP2>(o2); o3 += dpp<QP2>(o3);
                gemv32(red2, v2u{pk2(o0, o1), pk2(o2, o3)}, op, w, l);
            }
            bar();
            TS(8);
        } else {
            const u32 tag = epoch * 1664525u + 1013904223u;
            asm volatile("" :: "v"(pfd), "v"(tag));
            if (c < 4) { bar(); pub_bf16(ws + OFF_XH + (xcd * 4 + c) * 128, red32(red0, l), tag, l); TS(1); }
            bar();
            pub_bf16(ws + OFF_XQ + (g * 16 + c) * 128, red32(red1, l), tag, l);
            TS(4);
            {
                v4u vs[1]; const char* const pp[1] = {ws + OFF_XS + (g * 16 + (l & 15)) * 128 + ((l >> 4) & 1) * 16};
                gather<1>(vs, pp, tag);
                TS(5);
                float z0 = asf(vs[0].x), z1 = asf(vs[0].z), m0 = z0, m1 = z1;
                m0 = fmaxf(m0, dpp<QP1>(m0)); m1 = fmaxf(m1, dpp<QP1>(m1)); m0 = fmaxf(m0, dpp<QP2>(m0)); m1 = fmaxf(m1, dpp<QP2>(m1));
                m0 = fmaxf(m0, dpp<ROR(4)>(m0)); m1 = fmaxf(m1, dpp<ROR(4)>(m1)); m0 = fmaxf(m0, dpp<ROR(8)>(m0)); m1 = fmaxf(m1, dpp<ROR(8)>(m1));
                float e0 = __builtin_amdgcn_exp2f(z0 - m0), e1 = __builtin_amdgcn_exp2f(z1 - m1), s0 = e0, s1 = e1;
                s0 += dpp<QP1>(s0); s1 += dpp<QP1>(s1); s0 += dpp<QP2>(s0); s1 += dpp<QP2>(s1);
                s0 += dpp<ROR(4)>(s0); s1 += dpp<ROR(4)>(s1); s0 += dpp<ROR(8)>(s0); s1 += dpp<ROR(8)>(s1);
                if (l < 32) *(v2f*)(wt + (l & 15) * 4 + (l >> 4) * 2) = v2f{e1 / s1, e0 / s0};
            }
            bar();
            bar();
            {
                float r = red32(red2, l), r1 = dpp<QP1>(r);
                if (l < 32 && (l & 1) == 0) st16(ws + OFF_XW + (c * 8 + g) * 256 + l * 8, v4u{asu(r), tag, asu(r1), tag});
            }
            TS(8);
            if (c < 4) {
                const int col = (xcd * 4 + c) * 16 + 2 * (l & 7), pg = l >> 3;
                const u32 xr = *(const GAS u32*)(xin + col);
                v4u v3[3];
                const char* const pp[3] = {ws + OFF_XW + ((col >> 5) * 8 + pg) * 256 + (col & 31) * 8, ws + OFF_XM + (col & ~3) * 4, ws + OFF_XM + 6144 + (col & ~3) * 4};
                gather<3>(v3, pp, tag);
                float a0 = asf(v3[0].x), a1 = asf(v3[0].z);
                a0 += dpp<ROR(8)>(a0); a1 += dpp<ROR(8)>(a1); a0 = x32add(x16add(a0)); a1 = x32add(x16add(a1));
                const u32 ga = (l & 1) ? v3[1].z : v3[1].x, gb = (l & 1) ? v3[2].z : v3[2].x;
                const float x20 = rbf(lo16(xr) + rbf(lo16(ga) * rbf(a0))), x21 = rbf(hi16(xr) + rbf(hi16(ga) * rbf(a1)));
                TS(9);
                v4u vd[7];
                const char* base = ws + OFF_XD + pg * 4096 + col * 8;
                const char* const pd[7] = {base, base + 32768, base + 65536, base + 98304, base + 131072, base + 163840, base + 196608};
                gather<7>(vd, pd, tag);
                TS(10);
                float y0 = 0.f, y1 = 0.f;
#pragma unroll
                for (int i = 0; i < 7; ++i) { y0 += asf(vd[i].x); y1 += asf(vd[i].z); }
                y0 += dpp<ROR(8)>(y0); y1 += dpp<ROR(8)>(y1); y0 = x32add(x16add(y0)); y1 = x32add(x16add(y1));
                const u32 o = pk2(x20 + rbf(lo16(gb) * rbf(y0)), x21 + rbf(hi16(gb) * rbf(y1)));
                if (l < 8) *(GAS u32*)(out + col) = o;
                if (b == 128 && l == 0) __hip_atomic_store((GAS u32*)(ws + OFF_EP), tag, __ATOMIC_RELAXED, __HIP_MEMORY_SCOPE_AGENT);
                TS(11);
            }
        }
    } else {
        const int g = xcd, s = idx;
        const bool isM = idx < 7, isR = idx >= 7 && idx < 15;
        if (w < 8) {
            if (DLY_A) __builtin_amdgcn_s_sleep(DLY_A);
            v4u rk[8], rv[8];
            const size_t kb = ((size_t)g * 65536 + 61440 + 256 * s + 32 * w) * 256;
            {
                const char* p = (const char*)kc + kb + (l & 15) * 256 + (l >> 4) * 16;
#pragma unroll
                for (int i = 0; i < 8; ++i) rk[i] = ldnt(p + (i >> 2) * 4096 + (i & 3) * 64);
                const char* q = (const char*)vc + kb + (l >> 4) * 1024 + (l & 15) * 16;
#pragma unroll
                for (int i = 0; i < 8; ++i) rv[i] = ldnt(q + (i >> 2) * 4096 + (i & 3) * 256);
            }
            __builtin_amdgcn_sched_barrier(0);
            TS(1);
            bar();
            TS(2);
            v4f sa = {0.f, 0.f, 0.f, 0.f}, sb = sa;
#pragma unroll
            for (int t = 0; t < 4; ++t) {
                const v4u qv = *(const v4u*)(qs + (l & 3) * 64 + (4 * t + (l >> 4)) * 4);
                sa = mfma32(rk[t], qv, sa); sb = mfma32(rk[4 + t], qv, sb);
            }
            float e[8], mw;
#pragma unroll
            for (int i = 0; i < 4; ++i) { e[i] = rbf(rbf(sa[i]) * 0.08838834764831845f); e[4 + i] = rbf(rbf(sb[i]) * 0.08838834764831845f); }
            mw = fmaxf(fmaxf(fmaxf(e[0], e[1]), fmaxf(e[2], e[3])), fmaxf(fmaxf(e[4], e[5]), fmaxf(e[6], e[7])));
            mw = x32max(x16max(mw));
            float lw = 0.f;
#pragma unroll
            for (int i = 0; i < 8; ++i) { e[i] = __builtin_amdgcn_exp2f((e[i] - mw) * 1.4426950408889634f); lw += e[i]; }
            lw = x32add(x16add(lw));
            if (l < 4) *(v2f*)(st + l * 16 + w * 2) = v2f{mw, lw};
            TS(3);
            bar();
            v4u pa; float linv;
            {
                const v4f* q = (const v4f*)(st + (l & 3) * 16);
                const v4f s0 = q[0], s1 = q[1], s2 = q[2], s3 = q[3];
                const float mc = fmaxf(fmaxf(fmaxf(s0[0], s0[2]), fmaxf(s1[0], s1[2])), fmaxf(fmaxf(s2[0], s2[2]), fmaxf(s3[0], s3[2])));
                const float f = __builtin_amdgcn_exp2f((mw - mc) * 1.4426950408889634f);
                pa = v4u{pk2(e[0] * f, e[1] * f), pk2(e[2] * f, e[3] * f), pk2(e[4] * f, e[5] * f), pk2(e[6] * f, e[7] * f)};
                float lc = 0.f;
                lc += s0[1] * __builtin_amdgcn_exp2f((s0[0] - mc) * 1.4426950408889634f); lc += s0[3] * __builtin_amdgcn_exp2f((s0[2] - mc) * 1.4426950408889634f);
                lc += s1[1] * __builtin_amdgcn_exp2f((s1[0] - mc) * 1.4426950408889634f); lc += s1[3] * __builtin_amdgcn_exp2f((s1[2] - mc) * 1.4426950408889634f);
                lc += s2[1] * __builtin_amdgcn_exp2f((s2[0] - mc) * 1.4426950408889634f); lc += s2[3] * __builtin_amdgcn_exp2f((s2[2] - mc) * 1.4426950408889634f);
                lc += s3[1] * __builtin_amdgcn_exp2f((s3[0] - mc) * 1.4426950408889634f); lc += s3[3] * __builtin_amdgcn_exp2f((s3[2] - mc) * 1.4426950408889634f);
                linv = 1.f / lc;
            }
            const float li0 = dpp<0x00>(linv), li1 = dpp<0x55>(linv), li2 = dpp<0xAA>(linv), li3 = dpp<0xFF>(linv);
            TS(4);
            v4u vo[8];
#pragma unroll
            for (int pr = 0; pr < 4; ++pr)
#pragma unroll
                for (int j = 0; j < 8; ++j) vo[j][pr] = __builtin_amdgcn_perm(rv[2 * pr + 1][j >> 1], rv[2 * pr][j >> 1], (j & 1) ? 0x07060302u : 0x05040100u);
            TS(5);
            v4u r0[4], r1[4], r2[4]; v2u cv;
            const int jm = xcd * 7 + idx;
            if (isM) {
                const size_t o = (size_t)r4 * 3584 + jm * 64 + wofs;
#pragma unroll
                for (int i = 0; i < 4; ++i) r0[i] = ldnt((const char*)wg + o + i * 3584);
#pragma unroll
                for (int i = 0; i < 4; ++i) r1[i] = ldnt((const char*)wu + o + i * 3584);
                const char* p = (const char*)wd + (size_t)(32 * jm + 4 * (l >> 3)) * 1024 + 128 * w + (l & 7) * 16;
#pragma unroll
                for (int i = 0; i < 4; ++i) r2[i] = ldnt(p + i * 1024);
                cv = *(const GAS v2u*)(xin + r4);
            } else if (isR) {
                const char* p = (const char*)wmod + (size_t)r4 * 6144 + 2048 + (xcd * 8 + idx - 7) * 64 + wofs;
#pragma unroll
                for (int i = 0; i < 4; ++i) r0[i] = ldnt(p + i * 6144);
                cv = *(const GAS v2u*)(cin + r4);
            }
            __builtin_amdgcn_sched_barrier(0);
            {
                v4f d[8];
#pragma unroll
                for (int j = 0; j < 8; ++j) d[j] = mfma32(pa, vo[j], v4f{0.f, 0.f, 0.f, 0.f});
                if (l < 16) {
#pragma unroll
                    for (int j = 0; j < 8; ++j) po[(w * 8 + j) * 16 + l] = d[j];
                }
            }
            bar();
            const u32 tag = epoch * 1664525u + 1013904223u;
            if (w < 2) {
                v4f o = po[tid];
#pragma unroll
                for (int i = 1; i < 8; ++i) o += po[i * 128 + tid];
                const u32 l0 = pk2(o[0] * li0, o[2] * li2);
                const u32 b1 = asu(o[1] * li1), b3 = asu(o[3] * li3);
                const u32 q0 = l0 & 0xffffu, q2 = l0 >> 16;
                const u32 m1 = (b1 & 0x7fffffffu) + 0x8000u, m3 = (b3 & 0x7fffffffu) + 0x8000u;
                const u32 h1 = (m1 > q0 ? m1 - q0 : 0u) & 0x7fff0000u, h3 = (m3 > q2 ? m3 - q2 : 0u) & 0x7fff0000u;
                st16(ws + OFF_XA + (g * 16 + s) * 2048 + tid * 16, v4u{(b1 & 0x80000000u) | h1 | q0, tag, (b3 & 0x80000000u) | h3 | q2, tag});
            }
            TS(6);
            if (isR) {
                v2u op[8];
                v2u a = {pk2(silu_bf(lo16(cv.x)), silu_bf(hi16(cv.x))), pk2(silu_bf(lo16(cv.y)), silu_bf(hi16(cv.y)))};
                perm4(op, r0);
                gemv32(red0, a, op, w, l);
                bar();
                TS(7);
            } else if (isM) {
                v2u og[8], ou[8], od[8];
                perm4(og, r0); perm4(ou, r1); perm4(od, r2);
                TS(7);
                v4u vm[3];
                { const char* base = ws + OFF_XM + r4 * 4; const char* const pp[3] = {base, base + 2048, base + 4096}; gather<3>(vm, pp, tag); }
                TS(8);
                float x2[4];
                {
                    v4u vw[4];
                    const char* base = ws + OFF_XW + ((r4 >> 5) * 8 + 2 * (l & 3)) * 256 + (r4 & 31) * 8;
                    const char* const pp[4] = {base, base + 16, base + 256, base + 272};
                    gather<4>(vw, pp, tag);
                    TS(9);
                    float a0 = asf(vw[0].x) + asf(vw[2].x), a1 = asf(vw[0].z) + asf(vw[2].z), a2 = asf(vw[1].x) + asf(vw[3].x), a3 = asf(vw[1].z) + asf(vw[3].z);
                    a0 += dpp<QP1>(a0); a1 += dpp<QP1>(a1); a2 += dpp<QP1>(a2); a3 += dpp<QP1>(a3);
                    a0 += dpp<QP2>(a0); a1 += dpp<QP2>(a1); a2 += dpp<QP2>(a2); a3 += dpp<QP2>(a3);
                    x2[0] = rbf(lo16(cv.x) + rbf(lo16(vm[0].x) * rbf(a0))); x2[1] = rbf(hi16(cv.x) + rbf(hi16(vm[0].x) * rbf(a1)));
                    x2[2] = rbf(lo16(cv.y) + rbf(lo16(vm[0].z) * rbf(a2))); x2[3] = rbf(hi16(cv.y) + rbf(hi16(vm[0].z) * rbf(a3)));
                }
                {
                    const float pv = asf(__builtin_amdgcn_readfirstlane(asu(x2[0])));
                    const float d0 = x2[0] - pv, d1 = x2[1] - pv, d2 = x2[2] - pv, d3 = x2[3] - pv;
                    float s1 = (d0 + d1) + (d2 + d3), s2 = (d0 * d0 + d1 * d1) + (d2 * d2 + d3 * d3);
                    s1 += dpp<ROR(4)>(s1); s2 += dpp<ROR(4)>(s2); s1 += dpp<ROR(8)>(s1); s2 += dpp<ROR(8)>(s2);
                    s1 = x32add(x16add(s1)); s2 = x32add(x16add(s2));
                    if (l == 0) *(v4f*)(lns + w * 4) = v4f{pv, s1, s2, 0.f};
                }
                bar();
                {
                    const v4f t = *(const v4f*)(lns + (l & 7) * 4);
                    float a = 64.f * t[0] + t[1];
                    a += dpp<QP1>(a); a += dpp<QP2>(a); a += dpp<ROR(4)>(a) ;
                    const float mean = a * (1.f / 512.f), ee = mean - t[0];
                    float v = t[2] - 2.f * ee * t[1] + 64.f * ee * ee;
                    v += dpp<QP1>(v); v += dpp<QP2>(v); v += dpp<ROR(4)>(v);
                    const float mb = rbf(mean);
                    const float dn = rbf(__builtin_sqrtf(rbf(rbf(fmaxf(v, 0.f) * (1.f / 512.f)) + 1e-5f)));
                    const float ri = 1.f / dn;
                    v2u a2;
                    a2.x = pk2(rbf(rbf(rbf(x2[0] - mb) * ri) * rbf(1.f + lo16(vm[2].x))) + lo16(vm[1].x), rbf(rbf(rbf(x2[1] - mb) * ri) * rbf(1.f + hi16(vm[2].x))) + hi16(vm[1].x));
                    a2.y = pk2(rbf(rbf(rbf(x2[2] - mb) * ri) * rbf(1.f + lo16(vm[2].z))) + lo16(vm[1].z), rbf(rbf(rbf(x2[3] - mb) * ri) * rbf(1.f + hi16(vm[2].z))) + hi16(vm[1].z));
                    gemv32(red0, a2, og, w, l);
                    gemv32(red1, a2, ou, w, l);
                }
                bar();
                TS(10);
                {
                    const float* rr = (l < 32) ? red0 : red1;
                    const v4f t = *(const v4f*)(rr + (4 * w + ((l >> 3) & 3)) * 36 + (l & 7) * 4);
                    float a = (t[0] + t[1]) + (t[2] + t[3]);
                    a += dpp<QP1>(a); a += dpp<QP2>(a); a += dpp<0x141>(a);
                    auto r = __builtin_amdgcn_permlane32_swap(asu(a), asu(a), false, false);
                    const float gg = rbf(asf(r[0])), uu = rbf(asf(r[1]));
                    const float tt = 0.7978845608028654f * (gg + 0.044715f * gg * gg * gg);
                    const float th = 1.f - 2.f * __builtin_amdgcn_rcpf(1.f + __builtin_amdgcn_exp2f(tt * 2.885390081777927f));
                    const float hv = rbf(0.5f * gg * (1.f + th)) * uu;
                    const u32 hp = pk2(hv, dpp<ROR(8)>(hv));
                    if ((l & 47) == 0) hhs[2 * w + (l >> 4)] = hp;
                }
                bar();
                {
                    const v2u a = *(const v2u*)(hhs + 2 * (2 * (l >> 4) + (l & 1)));
                    float y[8];
#pragma unroll
                    for (int j = 0; j < 8; ++j) { const v4f d = mfma16(a, od[j]); y[j] = d[0] + dpp<ROR(8)>(d[1]); }
                    const int q = l >> 4;
                    const float y0 = q == 0 ? y[0] : q == 1 ? y[2] : q == 2 ? y[4] : y[6];
                    const float y1 = q == 0 ? y[1] : q == 1 ? y[3] : q == 2 ? y[5] : y[7];
                    if ((l & 8) == 0) st16(ws + OFF_XD + jm * 4096 + (64 * w + 8 * (l & 7) + 2 * q) * 8, v4u{asu(y0), tag, asu(y1), tag});
                }
                TS(11);
            }
        } else {
            const u32 tag = epoch * 1664525u + 1013904223u;
            asm volatile("" :: "v"(pfd), "v"(tag));
            {
                v4u v2[2]; const char* base = ws + OFF_XQ + g * 2048 + l * 16; const char* const pp[2] = {base, base + 1024};
                gather<2>(v2, pp, tag);
                *(v2u*)(qs + 2 * l) = v2u{v2[0].x, v2[0].z};
                *(v2u*)(qs + 128 + 2 * l) = v2u{v2[1].x, v2[1].z};
            }
            TS(2);
            bar();
            bar();
            {
                const v4f* q = (const v4f*)(st + (l & 3) * 16);
                const v4f s0 = q[0], s1 = q[1], s2 = q[2], s3 = q[3];
                const float mc = fmaxf(fmaxf(fmaxf(s0[0], s0[2]), fmaxf(s1[0], s1[2])), fmaxf(fmaxf(s2[0], s2[2]), fmaxf(s3[0], s3[2])));
                float lc = 0.f;
                lc += s0[1] * __builtin_amdgcn_exp2f((s0[0] - mc) * 1.4426950408889634f); lc += s0[3] * __builtin_amdgcn_exp2f((s0[2] - mc) * 1.4426950408889634f);
                lc += s1[1] * __builtin_amdgcn_exp2f((s1[0] - mc) * 1.4426950408889634f); lc += s1[3] * __builtin_amdgcn_exp2f((s1[2] - mc) * 1.4426950408889634f);
                lc += s2[1] * __builtin_amdgcn_exp2f((s2[0] - mc) * 1.4426950408889634f); lc += s2[3] * __builtin_amdgcn_exp2f((s2[2] - mc) * 1.4426950408889634f);
                lc += s3[1] * __builtin_amdgcn_exp2f((s3[0] - mc) * 1.4426950408889634f); lc += s3[3] * __builtin_amdgcn_exp2f((s3[2] - mc) * 1.4426950408889634f);
                const float z = mc * 1.4426950408889634f + __builtin_amdgcn_logf(lc);
                const float z1 = dpp<QP1>(z);
                if (l == 0 || l == 2) st16(ws + OFF_XS + (g * 16 + s) * 128 + l * 8, v4u{asu(z), tag, asu(z1), tag});
            }
            TS(4);
            bar();
            if (isR) { bar(); pub_bf16(ws + OFF_XM + (xcd * 8 + idx - 7) * 128, red32(red0, l), tag, l); TS(7); }
            else if (isM) { bar(); bar(); bar(); }
        }
    }
    TSDUMP();
}

extern "C" void kernel_launch(void* const* d_in, const int* in_sizes, int n_in, void* d_out, int out_size, void* d_ws, size_t ws_size, hipStream_t stream) {
    fused_kernel<<<256, 576, 0, stream>>>((const u16*)d_in[0], (const u16*)d_in[1], (const u16*)d_in[2], (const u16*)d_in[3], (const u16*)d_in[4],
        (const u16*)d_in[6], (const u16*)d_in[7], (const u16*)d_in[8], (const u16*)d_in[9], (const u16*)d_in[10], (u16*)d_out, (char*)d_ws);
}
```

```cpp
#include <hip/hip_runtime.h>
#include <stdint.h>

typedef unsigned u32;
typedef unsigned short u16;
typedef u32 v4u __attribute__((ext_vector_type(4)));
typedef u32 v2u __attribute__((ext_vector_type(2)));
typedef float v4f __attribute__((ext_vector_type(4)));
typedef float v2f __attribute__((ext_vector_type(2)));
typedef short v4s __attribute__((ext_vector_type(4)));
typedef __bf16 v8bf __attribute__((ext_vector_type(8)));
typedef __bf16 v2bf __attribute__((ext_vector_type(2)));
#define GAS __attribute__((address_space(1)))

#define DLY_A 0
#define DLY_WO 0

constexpr u32 OFF_EP = 0;
constexpr u32 OFF_XH = 4096;
constexpr u32 OFF_XM = 8192;
constexpr u32 OFF_XQ = 16384;
constexpr u32 OFF_XS = 32768;
constexpr u32 OFF_XW = 49152;
constexpr u32 OFF_XA = 131072;
constexpr u32 OFF_XD = 393216;
constexpr u32 OFF_DBG = 1u << 20;
constexpr int SPIN_MAX = 1 << 16;

__device__ __forceinline__ float asf(u32 v) { return __uint_as_float(v); }
__device__ __forceinline__ u32 asu(float v) { return __float_as_uint(v); }
__device__ __forceinline__ float lo16(u32 v) { return asf(v << 16); }
__device__ __forceinline__ float hi16(u32 v) { return asf(v & 0xffff0000u); }
__device__ __forceinline__ u32 pk2(float a, float b) { v2f t = {a, b}; v2bf r = __builtin_convertvector(t, v2bf); return __builtin_bit_cast(u32, r); }
__device__ __forceinline__ float rbf(float a) { return lo16(pk2(a, a) & 0xffffu); }
__device__ __forceinline__ v4u ldnt(const void* p) { return __builtin_nontemporal_load((const GAS v4u*)p); }
__device__ __forceinline__ void st16(void* p, v4u v) { asm volatile("global_store_dwordx4 %0, %1, off sc1\n s_nop 1" : : "v"(p), "v"(v) : "memory"); }
__device__ __forceinline__ v4u ld16(const void* p) { v4u v; asm volatile("global_load_dwordx4 %0, %1, off sc1" : "=v"(v) : "v"(p) : "memory"); return v; }
__device__ __forceinline__ void bar() { asm volatile("s_waitcnt lgkmcnt(0)\n s_barrier" ::: "memory"); }
template <int C> __device__ __forceinline__ float dpp(float x) { return asf(__builtin_amdgcn_update_dpp(0, asu(x), C, 0xf, 0xf, true)); }
#define QP1 0xB1
#define QP2 0x4E
#define ROR(n) (0x120 + (n))
__device__ __forceinline__ float x16add(float x) { auto r = __builtin_amdgcn_permlane16_swap(asu(x), asu(x), false, false); return asf(r[0]) + asf(r[1]); }
__device__ __forceinline__ float x32add(float x) { auto r = __builtin_amdgcn_permlane32_swap(asu(x), asu(x), false, false); return asf(r[0]) + asf(r[1]); }
__device__ __forceinline__ float x16max(float x) { auto r = __builtin_amdgcn_permlane16_swap(asu(x), asu(x), false, false); return fmaxf(asf(r[0]), asf(r[1])); }
__device__ __forceinline__ float x32max(float x) { auto r = __builtin_amdgcn_permlane32_swap(asu(x), asu(x), false, false); return fmaxf(asf(r[0]), asf(r[1])); }

template <int N> __device__ __forceinline__ void gather(v4u (&v)[N], const char* const (&p)[N], u32 tag) {
    for (int spin = 0; spin < SPIN_MAX; ++spin) {
#pragma unroll
        for (int i = 0; i < N; ++i) v[i] = ld16(p[i]);
        asm volatile("s_waitcnt vmcnt(0)" ::: "memory");
        u32 bad = 0;
#pragma unroll
        for (int i = 0; i < N; ++i) { asm volatile("" : "+v"(v[i])); bad |= (v[i].y ^ tag) | (v[i].w ^ tag); }
        if (__all(bad == 0)) break;
    }
}
__device__ __forceinline__ void perm4(v2u (&op)[8], const v4u (&r)[4]) {
#pragma unroll
    for (int j = 0; j < 8; ++j) {
        const u32 sel = (j & 1) ? 0x07060302u : 0x05040100u;
        op[j].x = __builtin_amdgcn_perm(r[1][j >> 1], r[0][j >> 1], sel);
        op[j].y = __builtin_amdgcn_perm(r[3][j >> 1], r[2][j >> 1], sel);
    }
}
__device__ __forceinline__ v4f mfma16(v2u a, v2u b) {
    return __builtin_amdgcn_mfma_f32_16x16x16bf16_1k(__builtin_bit_cast(v4s, a), __builtin_bit_cast(v4s, b), v4f{0.f, 0.f, 0.f, 0.f}, 0, 0, 0);
}
__device__ __forceinline__ v4f mfma32(v4u a, v4u b, v4f c) {
    return __builtin_amdgcn_mfma_f32_16x16x32_bf16(__builtin_bit_cast(v8bf, a), __builtin_bit_cast(v8bf, b), c, 0, 0, 0);
}
__device__ __forceinline__ void gemv32(float* red, v2u a, const v2u (&op)[8], int w, int l) {
    v4f d[8];
#pragma unroll
    for (int j = 0; j < 8; ++j) d[j] = mfma16(a, op[j]);
    if (((l >> 2) & 3) == (l >> 4)) {
        float* q = red + (8 * (l & 3)) * 36 + 4 * w + (l >> 4);
#pragma unroll
        for (int j = 0; j < 8; ++j) q[j * 36] = d[j][0];
    }
}
__device__ __forceinline__ float red32(const float* red, int l) {
    const v4f* q = (const v4f*)(red + (l & 31) * 36 + 16 * (l >> 5));
    v4f s = (q[0] + q[1]) + (q[2] + q[3]);
    return x32add((s[0] + s[1]) + (s[2] + s[3]));
}
__device__ __forceinline__ void pub_bf16(char* line, float r, u32 tag, int l) {
    float r1 = dpp<QP1>(r);
    u32 p = pk2(r, r1);
    u32 p2 = __builtin_amdgcn_update_dpp(0, p, QP2, 0xf, 0xf, true);
    if (l < 32 && (l & 3) == 0) st16(line + l * 4, v4u{p, tag, p2, tag});
}
__device__ __forceinline__ float silu_bf(float v) { return v * __builtin_amdgcn_rcpf(1.f + __expf(-v)); }

#define TS(i)
#define TSDUMP()

__global__ void __launch_bounds__(576) fused_kernel(const u16* __restrict__ xin, const u16* __restrict__ kc, const u16* __restrict__ vc,
        const u16* __restrict__ wq, const u16* __restrict__ wo, const u16* __restrict__ wg, const u16* __restrict__ wu,
        const u16* __restrict__ wd, const u16* __restrict__ cin, const u16* __restrict__ wmod, u16* __restrict__ out, char* __restrict__ ws) {
    __shared__ __attribute__((aligned(16))) float red0[32 * 36];
    __shared__ __attribute__((aligned(16))) float red1[32 * 36];
    __shared__ __attribute__((aligned(16))) float red2[32 * 36];
    __shared__ __attribute__((aligned(16))) v4f po[8 * 128];
    __shared__ __attribute__((aligned(16))) u32 qs[256];
    __shared__ __attribute__((aligned(16))) float st[64];
    __shared__ __attribute__((aligned(16))) float wt[64];
    __shared__ __attribute__((aligned(16))) float lns[32];
    __shared__ __attribute__((aligned(16))) u32 hhs[16];
    int b0 = blockIdx.x; int tid0 = threadIdx.x;
    const int w = __builtin_amdgcn_readfirstlane(tid0 >> 6);

    TS(0);
    const u32 epoch = __hip_atomic_load((const GAS u32*)(ws + OFF_EP), __ATOMIC_RELAXED, __HIP_MEMORY_SCOPE_AGENT);

    const uint64_t tst = __builtin_amdgcn_s_memrealtime();
    #pragma nounroll
    for (int it = 0; it < 2; ++it) {
    if (it) { while (__builtin_amdgcn_s_memrealtime() - tst < 4000) __builtin_amdgcn_s_sleep(1); }
    asm volatile("" : "+v"(tid0), "+s"(b0) :: "memory");
    const int b = b0; const int xcd = b & 7, idx = b >> 3;
    const int tid = tid0, l = tid & 63; const int r4 = 64 * w + (l & 60); const u32 wofs = (u32)(l & 3) * 16;
    if (idx >= 16) {
        const int c = idx - 16, g = (xcd + 1) & 7;
        if (w < 8) {
            v4u rm[4] = {}, rq[4]; v2u cv = {};
            const bool ismod = c < 4;
            if (ismod) {
                cv = *(const GAS v2u*)(cin + r4);
                const char* p = (const char*)wmod + (size_t)r4 * 6144 + (xcd * 4 + c) * 64 + wofs;
#pragma unroll
                for (int i = 0; i < 4; ++i) rm[i] = ldnt(p + i * 6144);
            }
            const v4u xa = *(const GAS v4u*)(xin + 8 * l);
            const v2u xo = *(const GAS v2u*)(xin + r4);
            {
                const char* p = (const char*)wq + (size_t)r4 * 8192 + (g * 512 + c * 32) * 2 + wofs;
#pragma unroll
                for (int i = 0; i < 4; ++i) rq[i] = ldnt(p + i * 8192);
            }
            __builtin_amdgcn_sched_barrier(0);
            v2u op[8];
            if (ismod) {
                v2u a = {pk2(silu_bf(lo16(cv.x)), silu_bf(hi16(cv.x))), pk2(silu_bf(lo16(cv.y)), silu_bf(hi16(cv.y)))};
                perm4(op, rm);
                gemv32(red0, a, op, w, l);
                TS(1);
                bar();
            }
            float xv[8] = {lo16(xa.x), hi16(xa.x), lo16(xa.y), hi16(xa.y), lo16(xa.z), hi16(xa.z), lo16(xa.w), hi16(xa.w)};
            float s = ((xv[0] + xv[1]) + (xv[2] + xv[3])) + ((xv[4] + xv[5]) + (xv[6] + xv[7]));
            s += dpp<QP1>(s); s += dpp<QP2>(s); s += dpp<ROR(4)>(s); s += dpp<ROR(8)>(s); s = x32add(x16add(s));
            const float mean = s * (1.f / 512.f);
            float v = 0.f;
#pragma unroll
            for (int i = 0; i < 8; ++i) { float d = xv[i] - mean; v += d * d; }
            v += dpp<QP1>(v); v += dpp<QP2>(v); v += dpp<ROR(4)>(v); v += dpp<ROR(8)>(v); v = x32add(x16add(v));
            const float mb = rbf(mean);
            const float dn = rbf(__builtin_sqrtf(rbf(rbf(v * (1.f / 512.f)) + 1e-5f)));
            float n[4] = {rbf(rbf(lo16(xo.x) - mb) / dn), rbf(rbf(hi16(xo.x) - mb) / dn), rbf(rbf(lo16(xo.y) - mb) / dn), rbf(rbf(hi16(xo.y) - mb) / dn)};
            perm4(op, rq);
            const u32 tag = epoch * 1664525u + 1013904223u + it;
            TS(2);
            {
                v4u v2[2]; const char* const pp[2] = {ws + OFF_XH + r4 * 4, ws + OFF_XH + 2048 + r4 * 4};
                gather<2>(v2, pp, tag);
                TS(3);
                v2u a;
                a.x = pk2(rbf(n[0] * rbf(1.f + lo16(v2[1].x))) + lo16(v2[0].x), rbf(n[1] * rbf(1.f + hi16(v2[1].x))) + hi16(v2[0].x));
                a.y = pk2(rbf(n[2] * rbf(1.f + lo16(v2[1].z))) + lo16(v2[0].z), rbf(n[3] * rbf(1.f + hi16(v2[1].z))) + hi16(v2[0].z));
                gemv32(red1, a, op, w, l);
            }
            bar();
            TS(4);
            if (DLY_WO) __builtin_amdgcn_s_sleep(DLY_WO);
            {
                const char* p = (const char*)wo + (size_t)(g * 512 + 8 * (l >> 2) + w) * 1024 + c * 64 + wofs;
#pragma unroll
                for (int i = 0; i < 4; ++i) rq[i] = ldnt(p + i * 131072);
            }
            __builtin_amdgcn_sched_barrier(0);
            bar();
            TS(5);
            v4f wv[4];
#pragma unroll
            for (int i = 0; i < 4; ++i) wv[i] = *(const v4f*)(wt + (4 * (l & 3) + i) * 4);
            perm4(op, rq);
            TS(6);
            {
                v4u va[4];
                const char* base = ws + OFF_XA + (g * 16 + 4 * (l & 3)) * 2048 + (16 * w + (l >> 2)) * 16;
                const char* const pp[4] = {base, base + 2048, base + 4096, base + 6144};
                gather<4>(va, pp, tag);
                TS(7);
                v2f a01 = {0.f, 0.f}, a23 = {0.f, 0.f};
#pragma unroll
                for (int i = 0; i < 4; ++i) {
                    a01 += v2f{asf(va[i].x), lo16(va[i].x)} * v2f{wv[i][0], wv[i][1]};
                    a23 += v2f{asf(va[i].z), lo16(va[i].z)} * v2f{wv[i][2], wv[i][3]};
                }
                float o1 = a01[0], o0 = a01[1], o3 = a23[0], o2 = a23[1];
                o0 += dpp<QP1>(o0); o1 += dpp<QP1>(o1); o2 += dpp<QP1>(o2); o3 += dpp<QP1>(o3);
                o0 += dpp<QP2>(o0); o1 += dpp<QP2>(o1); o2 += dpp<QP2>(o2); o3 += dpp<QP2>(o3);
                gemv32(red2, v2u{pk2(o0, o1), pk2(o2, o3)}, op, w, l);
            }
            bar();
            TS(8);
        } else {
            const u32 tag = epoch * 1664525u + 1013904223u + it;
            if (c < 4) { bar(); pub_bf16(ws + OFF_XH + (xcd * 4 + c) * 128, red32(red0, l), tag, l); TS(1); }
            bar();
            pub_bf16(ws + OFF_XQ + (g * 16 + c) * 128, red32(red1, l), tag, l);
            TS(4);
            {
                v4u vs[1]; const char* const pp[1] = {ws + OFF_XS + (g * 16 + (l & 15)) * 128 + ((l >> 4) & 1) * 16};
                gather<1>(vs, pp, tag);
                TS(5);
                float z0 = asf(vs[0].x), z1 = asf(vs[0].z), m0 = z0, m1 = z1;
                m0 = fmaxf(m0, dpp<QP1>(m0)); m1 = fmaxf(m1, dpp<QP1>(m1)); m0 = fmaxf(m0, dpp<QP2>(m0)); m1 = fmaxf(m1, dpp<QP2>(m1));
                m0 = fmaxf(m0, dpp<ROR(4)>(m0)); m1 = fmaxf(m1, dpp<ROR(4)>(m1)); m0 = fmaxf(m0, dpp<ROR(8)>(m0)); m1 = fmaxf(m1, dpp<ROR(8)>(m1));
                float e0 = __builtin_amdgcn_exp2f(z0 - m0), e1 = __builtin_amdgcn_exp2f(z1 - m1), s0 = e0, s1 = e1;
                s0 += dpp<QP1>(s0); s1 += dpp<QP1>(s1); s0 += dpp<QP2>(s0); s1 += dpp<QP2>(s1);
                s0 += dpp<ROR(4)>(s0); s1 += dpp<ROR(4)>(s1); s0 += dpp<ROR(8)>(s0); s1 += dpp<ROR(8)>(s1);
                if (l < 32) *(v2f*)(wt + (l & 15) * 4 + (l >> 4) * 2) = v2f{e1 / s1, e0 / s0};
            }
            bar();
            bar();
            {
                float r = red32(red2, l), r1 = dpp<QP1>(r);
                if (l < 32 && (l & 1) == 0) st16(ws + OFF_XW + (c * 8 + g) * 256 + l * 8, v4u{asu(r), tag, asu(r1), tag});
            }
            TS(8);
            if (c < 4) {
                const int col = (xcd * 4 + c) * 16 + 2 * (l & 7), pg = l >> 3;
                const u32 xr = *(const GAS u32*)(xin + col);
                v4u v3[3];
                const char* const pp[3] = {ws + OFF_XW + ((col >> 5) * 8 + pg) * 256 + (col & 31) * 8, ws + OFF_XM + (col & ~3) * 4, ws + OFF_XM + 6144 + (col & ~3) * 4};
                gather<3>(v3, pp, tag);
                float a0 = asf(v3[0].x), a1 = asf(v3[0].z);
                a0 += dpp<ROR(8)>(a0); a1 += dpp<ROR(8)>(a1); a0 = x32add(x16add(a0)); a1 = x32add(x16add(a1));
                const u32 ga = (l & 1) ? v3[1].z : v3[1].x, gb = (l & 1) ? v3[2].z : v3[2].x;
                const float x20 = rbf(lo16(xr) + rbf(lo16(ga) * rbf(a0))), x21 = rbf(hi16(xr) + rbf(hi16(ga) * rbf(a1)));
                TS(9);
                v4u vd[7];
                const char* base = ws + OFF_XD + pg * 4096 + col * 8;
                const char* const pd[7] = {base, base + 32768, base + 65536, base + 98304, base + 131072, base + 163840, base + 196608};
                gather<7>(vd, pd, tag);
                TS(10);
                float y0 = 0.f, y1 = 0.f;
#pragma unroll
                for (int i = 0; i < 7; ++i) { y0 += asf(vd[i].x); y1 += asf(vd[i].z); }
                y0 += dpp<ROR(8)>(y0); y1 += dpp<ROR(8)>(y1); y0 = x32add(x16add(y0)); y1 = x32add(x16add(y1));
                const u32 o = pk2(x20 + rbf(lo16(gb) * rbf(y0)), x21 + rbf(hi16(gb) * rbf(y1)));
                if (l < 8) *(GAS u32*)(out + col) = o;
                if (it && b == 128 && l == 0) __hip_atomic_store((GAS u32*)(ws + OFF_EP), tag, __ATOMIC_RELAXED, __HIP_MEMORY_SCOPE_AGENT);
                TS(11);
            }
        }
    } else {
        const int g = xcd, s = idx;
        const bool isM = idx < 7, isR = idx >= 7 && idx < 15;
        if (w < 8) {
            if (DLY_A) __builtin_amdgcn_s_sleep(DLY_A);
            v4u rk[8], rv[8];
            const size_t kb = ((size_t)g * 65536 + 61440 + 256 * s + 32 * w) * 256;
            {
                const char* p = (const char*)kc + kb + (l & 15) * 256 + (l >> 4) * 16;
#pragma unroll
                for (int i = 0; i < 8; ++i) rk[i] = ldnt(p + (i >> 2) * 4096 + (i & 3) * 64);
                const char* q = (const char*)vc + kb + (l >> 4) * 1024 + (l & 15) * 16;
#pragma unroll
                for (int i = 0; i < 8; ++i) rv[i] = ldnt(q + (i >> 2) * 4096 + (i & 3) * 256);
            }
            __builtin_amdgcn_sched_barrier(0);
            TS(1);
            bar();
            TS(2);
            v4f sa = {0.f, 0.f, 0.f, 0.f}, sb = sa;
#pragma unroll
            for (int t = 0; t < 4; ++t) {
                const v4u qv = *(const v4u*)(qs + (l & 3) * 64 + (4 * t + (l >> 4)) * 4);
                sa = mfma32(rk[t], qv, sa); sb = mfma32(rk[4 + t], qv, sb);
            }
            float e[8], mw;
#pragma unroll
            for (int i = 0; i < 4; ++i) { e[i] = rbf(rbf(sa[i]) * 0.08838834764831845f); e[4 + i] = rbf(rbf(sb[i]) * 0.08838834764831845f); }
            mw = fmaxf(fmaxf(fmaxf(e[0], e[1]), fmaxf(e[2], e[3])), fmaxf(fmaxf(e[4], e[5]), fmaxf(e[6], e[7])));
            mw = x32max(x16max(mw));
            float lw = 0.f;
#pragma unroll
            for (int i = 0; i < 8; ++i) { e[i] = __builtin_amdgcn_exp2f((e[i] - mw) * 1.4426950408889634f); lw += e[i]; }
            lw = x32add(x16add(lw));
            if (l < 4) *(v2f*)(st + l * 16 + w * 2) = v2f{mw, lw};
            TS(3);
            bar();
            v4u pa; float linv;
            {
                const v4f* q = (const v4f*)(st + (l & 3) * 16);
                const v4f s0 = q[0], s1 = q[1], s2 = q[2], s3 = q[3];
                const float mc = fmaxf(fmaxf(fmaxf(s0[0], s0[2]), fmaxf(s1[0], s1[2])), fmaxf(fmaxf(s2[0], s2[2]), fmaxf(s3[0], s3[2])));
                const float f = __builtin_amdgcn_exp2f((mw - mc) * 1.4426950408889634f);
                pa = v4u{pk2(e[0] * f, e[1] * f), pk2(e[2] * f, e[3] * f), pk2(e[4] * f, e[5] * f), pk2(e[6] * f, e[7] * f)};
                float lc = 0.f;
                lc += s0[1] * __builtin_amdgcn_exp2f((s0[0] - mc) * 1.4426950408889634f); lc += s0[3] * __builtin_amdgcn_exp2f((s0[2] - mc) * 1.4426950408889634f);
                lc += s1[1] * __builtin_amdgcn_exp2f((s1[0] - mc) * 1.4426950408889634f); lc += s1[3] * __builtin_amdgcn_exp2f((s1[2] - mc) * 1.4426950408889634f);
                lc += s2[1] * __builtin_amdgcn_exp2f((s2[0] - mc) * 1.4426950408889634f); lc += s2[3] * __builtin_amdgcn_exp2f((s2[2] - mc) * 1.4426950408889634f);
                lc += s3[1] * __builtin_amdgcn_exp2f((s3[0] - mc) * 1.4426950408889634f); lc += s3[3] * __builtin_amdgcn_exp2f((s3[2] - mc) * 1.4426950408889634f);
                linv = 1.f / lc;
            }
            const float li0 = dpp<0x00>(linv), li1 = dpp<0x55>(linv), li2 = dpp<0xAA>(linv), li3 = dpp<0xFF>(linv);
            TS(4);
            v4u vo[8];
#pragma unroll
            for (int pr = 0; pr < 4; ++pr)
#pragma unroll
                for (int j = 0; j < 8; ++j) vo[j][pr] = __builtin_amdgcn_perm(rv[2 * pr + 1][j >> 1], rv[2 * pr][j >> 1], (j & 1) ? 0x07060302u : 0x05040100u);
            TS(5);
            v4u r0[4] = {}, r1[4] = {}, r2[4] = {}; v2u cv = {};
            const int jm = xcd * 7 + idx;
            if (isM) {
                const size_t o = (size_t)r4 * 3584 + jm * 64 + wofs;
#pragma unroll
                for (int i = 0; i < 4; ++i) r0[i] = ldnt((const char*)wg + o + i * 3584);
#pragma unroll
                for (int i = 0; i < 4; ++i) r1[i] = ldnt((const char*)wu + o + i * 3584);
                const char* p = (const char*)wd + (size_t)(32 * jm + 4 * (l >> 3)) * 1024 + 128 * w + (l & 7) * 16;
#pragma unroll
                for (int i = 0; i < 4; ++i) r2[i] = ldnt(p + i * 1024);
                cv = *(const GAS v2u*)(xin + r4);
            } else if (isR) {
                const char* p = (const char*)wmod + (size_t)r4 * 6144 + 2048 + (xcd * 8 + idx - 7) * 64 + wofs;
#pragma unroll
                for (int i = 0; i < 4; ++i) r0[i] = ldnt(p + i * 6144);
                cv = *(const GAS v2u*)(cin + r4);
            }
            __builtin_amdgcn_sched_barrier(0);
            {
                v4f d[8];
#pragma unroll
                for (int j = 0; j < 8; ++j) d[j] = mfma32(pa, vo[j], v4f{0.f, 0.f, 0.f, 0.f});
                if (l < 16) {
#pragma unroll
                    for (int j = 0; j < 8; ++j) po[(w * 8 + j) * 16 + l] = d[j];
                }
            }
            bar();
            const u32 tag = epoch * 1664525u + 1013904223u + it;
            if (w < 2) {
                v4f o = po[tid];
#pragma unroll
                for (int i = 1; i < 8; ++i) o += po[i * 128 + tid];
                const u32 l0 = pk2(o[0] * li0, o[2] * li2);
                const u32 b1 = asu(o[1] * li1), b3 = asu(o[3] * li3);
                const u32 q0 = l0 & 0xffffu, q2 = l0 >> 16;
                const u32 m1 = (b1 & 0x7fffffffu) + 0x8000u, m3 = (b3 & 0x7fffffffu) + 0x8000u;
                const u32 h1 = (m1 > q0 ? m1 - q0 : 0u) & 0x7fff0000u, h3 = (m3 > q2 ? m3 - q2 : 0u) & 0x7fff0000u;
                st16(ws + OFF_XA + (g * 16 + s) * 2048 + tid * 16, v4u{(b1 & 0x80000000u) | h1 | q0, tag, (b3 & 0x80000000u) | h3 | q2, tag});
            }
            TS(6);
            if (isR) {
                v2u op[8];
                v2u a = {pk2(silu_bf(lo16(cv.x)), silu_bf(hi16(cv.x))), pk2(silu_bf(lo16(cv.y)), silu_bf(hi16(cv.y)))};
                perm4(op, r0);
                gemv32(red0, a, op, w, l);
                bar();
                TS(7);
            } else if (isM) {
                v2u og[8], ou[8], od[8];
                perm4(og, r0); perm4(ou, r1); perm4(od, r2);
                TS(7);
                v4u vm[3];
                { const char* base = ws + OFF_XM + r4 * 4; const char* const pp[3] = {base, base + 2048, base + 4096}; gather<3>(vm, pp, tag); }
                TS(8);
                float x2[4];
                {
                    v4u vw[4];
                    const char* base = ws + OFF_XW + ((r4 >> 5) * 8 + 2 * (l & 3)) * 256 + (r4 & 31) * 8;
                    const char* const pp[4] = {base, base + 16, base + 256, base + 272};
                    gather<4>(vw, pp, tag);
                    TS(9);
                    float a0 = asf(vw[0].x) + asf(vw[2].x), a1 = asf(vw[0].z) + asf(vw[2].z), a2 = asf(vw[1].x) + asf(vw[3].x), a3 = asf(vw[1].z) + asf(vw[3].z);
                    a0 += dpp<QP1>(a0); a1 += dpp<QP1>(a1); a2 += dpp<QP1>(a2); a3 += dpp<QP1>(a3);
                    a0 += dpp<QP2>(a0); a1 += dpp<QP2>(a1); a2 += dpp<QP2>(a2); a3 += dpp<QP2>(a3);
                    x2[0] = rbf(lo16(cv.x) + rbf(lo16(vm[0].x) * rbf(a0))); x2[1] = rbf(hi16(cv.x) + rbf(hi16(vm[0].x) * rbf(a1)));
                    x2[2] = rbf(lo16(cv.y) + rbf(lo16(vm[0].z) * rbf(a2))); x2[3] = rbf(hi16(cv.y) + rbf(hi16(vm[0].z) * rbf(a3)));
                }
                {
                    const float pv = asf(__builtin_amdgcn_readfirstlane(asu(x2[0])));
                    const float d0 = x2[0] - pv, d1 = x2[1] - pv, d2 = x2[2] - pv, d3 = x2[3] - pv;
                    float s1 = (d0 + d1) + (d2 + d3), s2 = (d0 * d0 + d1 * d1) + (d2 * d2 + d3 * d3);
                    s1 += dpp<ROR(4)>(s1); s2 += dpp<ROR(4)>(s2); s1 += dpp<ROR(8)>(s1); s2 += dpp<ROR(8)>(s2);
                    s1 = x32add(x16add(s1)); s2 = x32add(x16add(s2));
                    if (l == 0) *(v4f*)(lns + w * 4) = v4f{pv, s1, s2, 0.f};
                }
                bar();
                {
                    const v4f t = *(const v4f*)(lns + (l & 7) * 4);
                    float a = 64.f * t[0] + t[1];
                    a += dpp<QP1>(a); a += dpp<QP2>(a); a += dpp<ROR(4)>(a) ;
                    const float mean = a * (1.f / 512.f), ee = mean - t[0];
                    float v = t[2] - 2.f * ee * t[1] + 64.f * ee * ee;
                    v += dpp<QP1>(v); v += dpp<QP2>(v); v += dpp<ROR(4)>(v);
                    const float mb = rbf(mean);
                    const float dn = rbf(__builtin_sqrtf(rbf(rbf(fmaxf(v, 0.f) * (1.f / 512.f)) + 1e-5f)));
                    const float ri = 1.f / dn;
                    v2u a2;
                    a2.x = pk2(rbf(rbf(rbf(x2[0] - mb) * ri) * rbf(1.f + lo16(vm[2].x))) + lo16(vm[1].x), rbf(rbf(rbf(x2[1] - mb) * ri) * rbf(1.f + hi16(vm[2].x))) + hi16(vm[1].x));
                    a2.y = pk2(rbf(rbf(rbf(x2[2] - mb) * ri) * rbf(1.f + lo16(vm[2].z))) + lo16(vm[1].z), rbf(rbf(rbf(x2[3] - mb) * ri) * rbf(1.f + hi16(vm[2].z))) + hi16(vm[1].z));
                    gemv32(red0, a2, og, w, l);
                    gemv32(red1, a2, ou, w, l);
                }
                bar();
                TS(10);
                {
                    const float* rr = (l < 32) ? red0 : red1;
                    const v4f t = *(const v4f*)(rr + (4 * w + ((l >> 3) & 3)) * 36 + (l & 7) * 4);
                    float a = (t[0] + t[1]) + (t[2] + t[3]);
                    a += dpp<QP1>(a); a += dpp<QP2>(a); a += dpp<0x141>(a);
                    auto r = __builtin_amdgcn_permlane32_swap(asu(a), asu(a), false, false);
                    const float gg = rbf(asf(r[0])), uu = rbf(asf(r[1]));
                    const float tt = 0.7978845608028654f * (gg + 0.044715f * gg * gg * gg);
                    const float th = 1.f - 2.f * __builtin_amdgcn_rcpf(1.f + __builtin_amdgcn_exp2f(tt * 2.885390081777927f));
                    const float hv = rbf(0.5f * gg * (1.f + th)) * uu;
                    const u32 hp = pk2(hv, dpp<ROR(8)>(hv));
                    if ((l & 47) == 0) hhs[2 * w + (l >> 4)] = hp;
                }
                bar();
                {
                    const v2u a = *(const v2u*)(hhs + 2 * (2 * (l >> 4) + (l & 1)));
                    float y[8];
#pragma unroll
                    for (int j = 0; j < 8; ++j) { const v4f d = mfma16(a, od[j]); y[j] = d[0] + dpp<ROR(8)>(d[1]); }
                    const int q = l >> 4;
                    const float y0 = q == 0 ? y[0] : q == 1 ? y[2] : q == 2 ? y[4] : y[6];
                    const float y1 = q == 0 ? y[1] : q == 1 ? y[3] : q == 2 ? y[5] : y[7];
                    if ((l & 8) == 0) st16(ws + OFF_XD + jm * 4096 + (64 * w + 8 * (l & 7) + 2 * q) * 8, v4u{asu(y0), tag, asu(y1), tag});
                }
                TS(11);
            }
        } else {
            const u32 tag = epoch * 1664525u + 1013904223u + it;
            {
                v4u v2[2]; const char* base = ws + OFF_XQ + g * 2048 + l * 16; const char* const pp[2] = {base, base + 1024};
                gather<2>(v2, pp, tag);
                *(v2u*)(qs + 2 * l) = v2u{v2[0].x, v2[0].z};
                *(v2u*)(qs + 128 + 2 * l) = v2u{v2[1].x, v2[1].z};
            }
            TS(2);
            bar();
            bar();
            {
                const v4f* q = (const v4f*)(st + (l & 3) * 16);
                const v4f s0 = q[0], s1 = q[1], s2 = q[2], s3 = q[3];
                const float mc = fmaxf(fmaxf(fmaxf(s0[0], s0[2]), fmaxf(s1[0], s1[2])), fmaxf(fmaxf(s2[0], s2[2]), fmaxf(s3[0], s3[2])));
                float lc = 0.f;
                lc += s0[1] * __builtin_amdgcn_exp2f((s0[0] - mc) * 1.4426950408889634f); lc += s0[3] * __builtin_amdgcn_exp2f((s0[2] - mc) * 1.4426950408889634f);
                lc += s1[1] * __builtin_amdgcn_exp2f((s1[0] - mc) * 1.4426950408889634f); lc += s1[3] * __builtin_amdgcn_exp2f((s1[2] - mc) * 1.4426950408889634f);
                lc += s2[1] * __builtin_amdgcn_exp2f((s2[0] - mc) * 1.4426950408889634f); lc += s2[3] * __builtin_amdgcn_exp2f((s2[2] - mc) * 1.4426950408889634f);
                lc += s3[1] * __builtin_amdgcn_exp2f((s3[0] - mc) * 1.4426950408889634f); lc += s3[3] * __builtin_amdgcn_exp2f((s3[2] - mc) * 1.4426950408889634f);
                const float z = mc * 1.4426950408889634f + __builtin_amdgcn_logf(lc);
                const float z1 = dpp<QP1>(z);
                if (l == 0 || l == 2) st16(ws + OFF_XS + (g * 16 + s) * 128 + l * 8, v4u{asu(z), tag, asu(z1), tag});
            }
            TS(4);
            bar();
            if (isR) { bar(); pub_bf16(ws + OFF_XM + (xcd * 8 + idx - 7) * 128, red32(red0, l), tag, l); TS(7); }
            else if (isM) { bar(); bar(); bar(); }
        }
    }
    }
    TSDUMP();
}

extern "C" void kernel_launch(void* const* d_in, const int* in_sizes, int n_in, void* d_out, int out_size, void* d_ws, size_t ws_size, hipStream_t stream) {
    fused_kernel<<<256, 576, 0, stream>>>((const u16*)d_in[0], (const u16*)d_in[1], (const u16*)d_in[2], (const u16*)d_in[3], (const u16*)d_in[4],
        (const u16*)d_in[6], (const u16*)d_in[7], (const u16*)d_in[8], (const u16*)d_in[9], (const u16*)d_in[10], (u16*)d_out, (char*)d_ws);
}
```

```cpp
#include <hip/hip_runtime.h>
#include <stdint.h>

typedef unsigned u32;
typedef unsigned short u16;
typedef u32 v4u __attribute__((ext_vector_type(4)));
typedef u32 v2u __attribute__((ext_vector_type(2)));
typedef float v4f __attribute__((ext_vector_type(4)));
typedef float v2f __attribute__((ext_vector_type(2)));
typedef short v4s __attribute__((ext_vector_type(4)));
typedef __bf16 v8bf __attribute__((ext_vector_type(8)));
typedef __bf16 v2bf __attribute__((ext_vector_type(2)));
#define GAS __attribute__((address_space(1)))

#define PF_N 8
#define PF_L 32
#define DLY_A 0
#define DLY_WO 0

constexpr u32 OFF_EP = 0;
constexpr u32 OFF_XH = 4096;
constexpr u32 OFF_XM = 8192;
constexpr u32 OFF_XQ = 16384;
constexpr u32 OFF_XS = 32768;
constexpr u32 OFF_XW = 49152;
constexpr u32 OFF_XA = 131072;
constexpr u32 OFF_XD = 393216;
constexpr u32 OFF_DBG = 1u << 20;
constexpr int SPIN_MAX = 1 << 16;

__device__ __forceinline__ float asf(u32 v) { return __uint_as_float(v); }
__device__ __forceinline__ u32 asu(float v) { return __float_as_uint(v); }
__device__ __forceinline__ float lo16(u32 v) { return asf(v << 16); }
__device__ __forceinline__ float hi16(u32 v) { return asf(v & 0xffff0000u); }
__device__ __forceinline__ u32 pk2(float a, float b) { v2f t = {a, b}; v2bf r = __builtin_convertvector(t, v2bf); return __builtin_bit_cast(u32, r); }
__device__ __forceinline__ float rbf(float a) { return lo16(pk2(a, a) & 0xffffu); }
__device__ __forceinline__ v4u ldnt(const void* p) { return __builtin_nontemporal_load((const GAS v4u*)p); }
__device__ __forceinline__ void st16(void* p, v4u v) { asm volatile("global_store_dwordx4 %0, %1, off sc1\n s_nop 1" : : "v"(p), "v"(v) : "memory"); }
__device__ __forceinline__ v4u ld16(const void* p) { v4u v; asm volatile("global_load_dwordx4 %0, %1, off sc1" : "=v"(v) : "v"(p) : "memory"); return v; }
__device__ __forceinline__ void bar() { asm volatile("s_waitcnt lgkmcnt(0)\n s_barrier" ::: "memory"); }
template <int C> __device__ __forceinline__ float dpp(float x) { return asf(__builtin_amdgcn_update_dpp(0, asu(x), C, 0xf, 0xf, true)); }
#define QP1 0xB1
#define QP2 0x4E
#define ROR(n) (0x120 + (n))
__device__ __forceinline__ float x16add(float x) { auto r = __builtin_amdgcn_permlane16_swap(asu(x), asu(x), false, false); return asf(r[0]) + asf(r[1]); }
__device__ __forceinline__ float x32add(float x) { auto r = __builtin_amdgcn_permlane32_swap(asu(x), asu(x), false, false); return asf(r[0]) + asf(r[1]); }
__device__ __forceinline__ float x16max(float x) { auto r = __builtin_amdgcn_permlane16_swap(asu(x), asu(x), false, false); return fmaxf(asf(r[0]), asf(r[1])); }
__device__ __forceinline__ float x32max(float x) { auto r = __builtin_amdgcn_permlane32_swap(asu(x), asu(x), false, false); return fmaxf(asf(r[0]), asf(r[1])); }

template <int N> __device__ __forceinline__ void gather(v4u (&v)[N], const char* const (&p)[N], u32 tag) {
    for (int spin = 0; spin < SPIN_MAX; ++spin) {
#pragma unroll
        for (int i = 0; i < N; ++i) v[i] = ld16(p[i]);
        asm volatile("s_waitcnt vmcnt(0)" ::: "memory");
        u32 bad = 0;
#pragma unroll
        for (int i = 0; i < N; ++i) { asm volatile("" : "+v"(v[i])); bad |= (v[i].y ^ tag) | (v[i].w ^ tag); }
        if (__all(bad == 0)) break;
    }
}
__device__ __forceinline__ void perm4(v2u (&op)[8], const v4u (&r)[4]) {
#pragma unroll
    for (int j = 0; j < 8; ++j) {
        const u32 sel = (j & 1) ? 0x07060302u : 0x05040100u;
        op[j].x = __builtin_amdgcn_perm(r[1][j >> 1], r[0][j >> 1], sel);
        op[j].y = __builtin_amdgcn_perm(r[3][j >> 1], r[2][j >> 1], sel);
    }
}
__device__ __forceinline__ v4f mfma16(v2u a, v2u b) {
    return __builtin_amdgcn_mfma_f32_16x16x16bf16_1k(__builtin_bit_cast(v4s, a), __builtin_bit_cast(v4s, b), v4f{0.f, 0.f, 0.f, 0.f}, 0, 0, 0);
}
__device__ __forceinline__ v4f mfma32(v4u a, v4u b, v4f c) {
    return __builtin_amdgcn_mfma_f32_16x16x32_bf16(__builtin_bit_cast(v8bf, a), __builtin_bit_cast(v8bf, b), c, 0, 0, 0);
}
__device__ __forceinline__ void gemv32(float* red, v2u a, const v2u (&op)[8], int w, int l) {
    v4f d[8];
#pragma unroll
    for (int j = 0; j < 8; ++j) d[j] = mfma16(a, op[j]);
    if (((l >> 2) & 3) == (l >> 4)) {
        float* q = red + (8 * (l & 3)) * 36 + 4 * w + (l >> 4);
#pragma unroll
        for (int j = 0; j < 8; ++j) q[j * 36] = d[j][0];
    }
}
__device__ __forceinline__ float red32(const float* red, int l) {
    const v4f* q = (const v4f*)(red + (l & 31) * 36 + 16 * (l >> 5));
    v4f s = (q[0] + q[1]) + (q[2] + q[3]);
    return x32add((s[0] + s[1]) + (s[2] + s[3]));
}
__device__ __forceinline__ void pub_bf16(char* line, float r, u32 tag, int l) {
    float r1 = dpp<QP1>(r);
    u32 p = pk2(r, r1);
    u32 p2 = __builtin_amdgcn_update_dpp(0, p, QP2, 0xf, 0xf, true);
    if (l < 32 && (l & 3) == 0) st16(line + l * 4, v4u{p, tag, p2, tag});
}
__device__ __forceinline__ float silu_bf(float v) { return v * __builtin_amdgcn_rcpf(1.f + __expf(-v)); }

#define TS(i)
#define TSDUMP()

typedef u32 v16u __attribute__((ext_vector_type(16)));
typedef u32 v8u __attribute__((ext_vector_type(8)));
#define MKP(T, lo, hi) ((T)(((uint64_t)(hi) << 32) | (lo)))
__global__ void __launch_bounds__(576) fused_kernel(const u16* a0, const u16* a1, const u16* a2, const u16* a3, const u16* a4, const u16* a5, const u16* a6,
        const u16* a7, const u16* a8, const u16* a9, u16* a10, char* a11) {
    v16u ka; v8u kb_;
    asm volatile("s_load_dwordx16 %0, %2, 0\n s_load_dwordx8 %1, %2, 64\n s_waitcnt lgkmcnt(0)" : "=&s"(ka), "=&s"(kb_) : "s"(__builtin_amdgcn_kernarg_segment_ptr()));
    const u16* __restrict__ xin = MKP(const u16*, ka[0], ka[1]); const u16* __restrict__ kc = MKP(const u16*, ka[2], ka[3]); const u16* __restrict__ vc = MKP(const u16*, ka[4], ka[5]);
    const u16* __restrict__ wq = MKP(const u16*, ka[6], ka[7]); const u16* __restrict__ wo = MKP(const u16*, ka[8], ka[9]); const u16* __restrict__ wg = MKP(const u16*, ka[10], ka[11]);
    const u16* __restrict__ wu = MKP(const u16*, ka[12], ka[13]); const u16* __restrict__ wd = MKP(const u16*, ka[14], ka[15]); const u16* __restrict__ cin = MKP(const u16*, kb_[0], kb_[1]);
    const u16* __restrict__ wmod = MKP(const u16*, kb_[2], kb_[3]); u16* __restrict__ out = MKP(u16*, kb_[4], kb_[5]); char* __restrict__ ws = MKP(char*, kb_[6], kb_[7]);
    __shared__ __attribute__((aligned(16))) float red0[32 * 36];
    __shared__ __attribute__((aligned(16))) float red1[32 * 36];
    __shared__ __attribute__((aligned(16))) float red2[32 * 36];
    __shared__ __attribute__((aligned(16))) v4f po[8 * 128];
    __shared__ __attribute__((aligned(16))) u32 qs[256];
    __shared__ __attribute__((aligned(16))) float st[64];
    __shared__ __attribute__((aligned(16))) float wt[64];
    __shared__ __attribute__((aligned(16))) float lns[32];
    __shared__ __attribute__((aligned(16))) u32 hhs[16];
    const int b = blockIdx.x, tid = threadIdx.x, l = tid & 63;
    const int w = __builtin_amdgcn_readfirstlane(tid >> 6);
    const int xcd = b & 7, idx = b >> 3;
    TS(0);

    u32 pfd = 0;
    if (w == 8) {
        uint64_t pc; u32 len;
        asm volatile("s_getpc_b64 %0" : "=s"(pc)); len = 15000;
        const u32 off = (u32)((idx & (PF_N - 1)) * PF_L + (l & (PF_L - 1))) * 128u;
        if (l < PF_L && off < len) asm volatile("global_load_dword %0, %1, off" : "+v"(pfd) : "v"(pc + off) : "memory");
    }
    const u32 epoch = __hip_atomic_load((const GAS u32*)(ws + OFF_EP), __ATOMIC_RELAXED, __HIP_MEMORY_SCOPE_AGENT);
    const int r4 = 64 * w + (l & 60);
    const u32 wofs = (u32)(l & 3) * 16;

    if (idx >= 16) {
        const int c = idx - 16, g = (xcd + 1) & 7;
        if (w < 8) {
            v4u rm[4], rq[4]; v2u cv;
            const bool ismod = c < 4;
            if (ismod) {
                cv = *(const GAS v2u*)(cin + r4);
                const char* p = (const char*)wmod + (size_t)r4 * 6144 + (xcd * 4 + c) * 64 + wofs;
#pragma unroll
                for (int i = 0; i < 4; ++i) rm[i] = ldnt(p + i * 6144);
            }
            const v4u xa = *(const GAS v4u*)(xin + 8 * l);
            const v2u xo = *(const GAS v2u*)(xin + r4);
            {
                const char* p = (const char*)wq + (size_t)r4 * 8192 + (g * 512 + c * 32) * 2 + wofs;
#pragma unroll
                for (int i = 0; i < 4; ++i) rq[i] = ldnt(p + i * 8192);
            }
            __builtin_amdgcn_sched_barrier(0);
            v2u op[8];
            if (ismod) {
                v2u a = {pk2(silu_bf(lo16(cv.x)), silu_bf(hi16(cv.x))), pk2(silu_bf(lo16(cv.y)), silu_bf(hi16(cv.y)))};
                perm4(op, rm);
                gemv32(red0, a, op, w, l);
                TS(1);
                bar();
            }
            float xv[8] = {lo16(xa.x), hi16(xa.x), lo16(xa.y), hi16(xa.y), lo16(xa.z), hi16(xa.z), lo16(xa.w), hi16(xa.w)};
            float s = ((xv[0] + xv[1]) + (xv[2] + xv[3])) + ((xv[4] + xv[5]) + (xv[6] + xv[7]));
            s += dpp<QP1>(s); s += dpp<QP2>(s); s += dpp<ROR(4)>(s); s += dpp<ROR(8)>(s); s = x32add(x16add(s));
            const float mean = s * (1.f / 512.f);
            float v = 0.f;
#pragma unroll
            for (int i = 0; i < 8; ++i) { float d = xv[i] - mean; v += d * d; }
            v += dpp<QP1>(v); v += dpp<QP2>(v); v += dpp<ROR(4)>(v); v += dpp<ROR(8)>(v); v = x32add(x16add(v));
            const float mb = rbf(mean);
            const float dn = rbf(__builtin_sqrtf(rbf(rbf(v * (1.f / 512.f)) + 1e-5f)));
            float n[4] = {rbf(rbf(lo16(xo.x) - mb) / dn), rbf(rbf(hi16(xo.x) - mb) / dn), rbf(rbf(lo16(xo.y) - mb) / dn), rbf(rbf(hi16(xo.y) - mb) / dn)};
            perm4(op, rq);
            const u32 tag = epoch * 1664525u + 1013904223u;
            TS(2);
            {
                v4u v2[2]; const char* const pp[2] = {ws + OFF_XH + r4 * 4, ws + OFF_XH + 2048 + r4 * 4};
                gather<2>(v2, pp, tag);
                TS(3);
                v2u a;
                a.x = pk2(rbf(n[0] * rbf(1.f + lo16(v2[1].x))) + lo16(v2[0].x), rbf(n[1] * rbf(1.f + hi16(v2[1].x))) + hi16(v2[0].x));
                a.y = pk2(rbf(n[2] * rbf(1.f + lo16(v2[1].z))) + lo16(v2[0].z), rbf(n[3] * rbf(1.f + hi16(v2[1].z))) + hi16(v2[0].z));
                gemv32(red1, a, op, w, l);
            }
            bar();
            TS(4);
            if (DLY_WO) __builtin_amdgcn_s_sleep(DLY_WO);
            {
                const char* p = (const char*)wo + (size_t)(g * 512 + 8 * (l >> 2) + w) * 1024 + c * 64 + wofs;
#pragma unroll
                for (int i = 0; i < 4; ++i) rq[i] = ldnt(p + i * 131072);
            }
            __builtin_amdgcn_sched_barrier(0);
            bar();
            TS(5);
            v4f wv[4];
#pragma unroll
            for (int i = 0; i < 4; ++i) wv[i] = *(const v4f*)(wt + (4 * (l & 3) + i) * 4);
            perm4(op, rq);
            TS(6);
            {
                v4u va[4];
                const char* base = ws + OFF_XA + (g * 16 + 4 * (l & 3)) * 2048 + (16 * w + (l >> 2)) * 16;
                const char* const pp[4] = {base, base + 2048, base + 4096, base + 6144};
                gather<4>(va, pp, tag);
                TS(7);
                v2f a01 = {0.f, 0.f}, a23 = {0.f, 0.f};
#pragma unroll
                for (int i = 0; i < 4; ++i) {
                    a01 += v2f{asf(va[i].x), lo16(va[i].x)} * v2f{wv[i][0], wv[i][1]};
                    a23 += v2f{asf(va[i].z), lo16(va[i].z)} * v2f{wv[i][2], wv[i][3]};
                }
                float o1 = a01[0], o0 = a01[1], o3 = a23[0], o2 = a23[1];
                o0 += dpp<QP1>(o0); o1 += dpp<QP1>(o1); o2 += dpp<QP1>(o2); o3 += dpp<QP1>(o3);
                o0 += dpp<QP2>(o0); o1 += dpp<QP2>(o1); o2 += dpp<QP2>(o2); o3 += dpp<QP2>(o3);
                gemv32(red2, v2u{pk2(o0, o1), pk2(o2, o3)}, op, w, l);
            }
            bar();
            TS(8);
        } else {
            const u32 tag = epoch * 1664525u + 1013904223u;
            asm volatile("" :: "v"(pfd), "v"(tag));
            if (c < 4) { bar(); pub_bf16(ws + OFF_XH + (xcd * 4 + c) * 128, red32(red0, l), tag, l); TS(1); }
            bar();
            pub_bf16(ws + OFF_XQ + (g * 16 + c) * 128, red32(red1, l), tag, l);
            TS(4);
            {
                v4u vs[1]; const char* const pp[1] = {ws + OFF_XS + (g * 16 + (l & 15)) * 128 + ((l >> 4) & 1) * 16};
                gather<1>(vs, pp, tag);
                TS(5);
                float z0 = asf(vs[0].x), z1 = asf(vs[0].z), m0 = z0, m1 = z1;
                m0 = fmaxf(m0, dpp<QP1>(m0)); m1 = fmaxf(m1, dpp<QP1>(m1)); m0 = fmaxf(m0, dpp<QP2>(m0)); m1 = fmaxf(m1, dpp<QP2>(m1));
                m0 = fmaxf(m0, dpp<ROR(4)>(m0)); m1 = fmaxf(m1, dpp<ROR(4)>(m1)); m0 = fmaxf(m0, dpp<ROR(8)>(m0)); m1 = fmaxf(m1, dpp<ROR(8)>(m1));
                float e0 = __builtin_amdgcn_exp2f(z0 - m0), e1 = __builtin_amdgcn_exp2f(z1 - m1), s0 = e0, s1 = e1;
                s0 += dpp<QP1>(s0); s1 += dpp<QP1>(s1); s0 += dpp<QP2>(s0); s1 += dpp<QP2>(s1);
                s0 += dpp<ROR(4)>(s0); s1 += dpp<ROR(4)>(s1); s0 += dpp<ROR(8)>(s0); s1 += dpp<ROR(8)>(s1);
                if (l < 32) *(v2f*)(wt + (l & 15) * 4 + (l >> 4) * 2) = v2f{e1 / s1, e0 / s0};
            }
            bar();
            bar();
            {
                float r = red32(red2, l), r1 = dpp<QP1>(r);
                if (l < 32 && (l & 1) == 0) st16(ws + OFF_XW + (c * 8 + g) * 256 + l * 8, v4u{asu(r), tag, asu(r1), tag});
            }
            TS(8);
            if (c < 4) {
                const int col = (xcd * 4 + c) * 16 + 2 * (l & 7), pg = l >> 3;
                const u32 xr = *(const GAS u32*)(xin + col);
                v4u v3[3];
                const char* const pp[3] = {ws + OFF_XW + ((col >> 5) * 8 + pg) * 256 + (col & 31) * 8, ws + OFF_XM + (col & ~3) * 4, ws + OFF_XM + 6144 + (col & ~3) * 4};
                gather<3>(v3, pp, tag);
                float a0 = asf(v3[0].x), a1 = asf(v3[0].z);
                a0 += dpp<ROR(8)>(a0); a1 += dpp<ROR(8)>(a1); a0 = x32add(x16add(a0)); a1 = x32add(x16add(a1));
                const u32 ga = (l & 1) ? v3[1].z : v3[1].x, gb = (l & 1) ? v3[2].z : v3[2].x;
                const float x20 = rbf(lo16(xr) + rbf(lo16(ga) * rbf(a0))), x21 = rbf(hi16(xr) + rbf(hi16(ga) * rbf(a1)));
                TS(9);
                v4u vd[7];
                const char* base = ws + OFF_XD + pg * 4096 + col * 8;
                const char* const pd[7] = {base, base + 32768, base + 65536, base + 98304, base + 131072, base + 163840, base + 196608};
                gather<7>(vd, pd, tag);
                TS(10);
                float y0 = 0.f, y1 = 0.f;
#pragma unroll
                for (int i = 0; i < 7; ++i) { y0 += asf(vd[i].x); y1 += asf(vd[i].z); }
                y0 += dpp<ROR(8)>(y0); y1 += dpp<ROR(8)>(y1); y0 = x32add(x16add(y0)); y1 = x32add(x16add(y1));
                const u32 o = pk2(x20 + rbf(lo16(gb) * rbf(y0)), x21 + rbf(hi16(gb) * rbf(y1)));
                if (l < 8) *(GAS u32*)(out + col) = o;
                if (b == 128 && l == 0) __hip_atomic_store((GAS u32*)(ws + OFF_EP), tag, __ATOMIC_RELAXED, __HIP_MEMORY_SCOPE_AGENT);
                TS(11);
            }
        }
    } else {
        const int g = xcd, s = idx;
        const bool isM = idx < 7, isR = idx >= 7 && idx < 15;
        if (w < 8) {
            if (DLY_A) __builtin_amdgcn_s_sleep(DLY_A);
            v4u rk[8], rv[8];
            const size_t kb = ((size_t)g * 65536 + 61440 + 256 * s + 32 * w) * 256;
            {
                const char* p = (const char*)kc + kb + (l & 15) * 256 + (l >> 4) * 16;
#pragma unroll
                for (int i = 0; i < 8; ++i) rk[i] = ldnt(p + (i >> 2) * 4096 + (i & 3) * 64);
                const char* q = (const char*)vc + kb + (l >> 4) * 1024 + (l & 15) * 16;
#pragma unroll
                for (int i = 0; i < 8; ++i) rv[i] = ldnt(q + (i >> 2) * 4096 + (i & 3) * 256);
            }
            __builtin_amdgcn_sched_barrier(0);
            TS(1);
            bar();
            TS(2);
            v4f sa = {0.f, 0.f, 0.f, 0.f}, sb = sa;
#pragma unroll
            for (int t = 0; t < 4; ++t) {
                const v4u qv = *(const v4u*)(qs + (l & 3) * 64 + (4 * t + (l >> 4)) * 4);
                sa = mfma32(rk[t], qv, sa); sb = mfma32(rk[4 + t], qv, sb);
            }
            float e[8], mw;
#pragma unroll
            for (int i = 0; i < 4; ++i) { e[i] = rbf(rbf(sa[i]) * 0.08838834764831845f); e[4 + i] = rbf(rbf(sb[i]) * 0.08838834764831845f); }
            mw = fmaxf(fmaxf(fmaxf(e[0], e[1]), fmaxf(e[2], e[3])), fmaxf(fmaxf(e[4], e[5]), fmaxf(e[6], e[7])));
            mw = x32max(x16max(mw));
            float lw = 0.f;
#pragma unroll
            for (int i = 0; i < 8; ++i) { e[i] = __builtin_amdgcn_exp2f((e[i] - mw) * 1.4426950408889634f); lw += e[i]; }
            lw = x32add(x16add(lw));
            if (l < 4) *(v2f*)(st + l * 16 + w * 2) = v2f{mw, lw};
            TS(3);
            bar();
            v4u pa; float linv;
            {
                const v4f* q = (const v4f*)(st + (l & 3) * 16);
                const v4f s0 = q[0], s1 = q[1], s2 = q[2], s3 = q[3];
                const float mc = fmaxf(fmaxf(fmaxf(s0[0], s0[2]), fmaxf(s1[0], s1[2])), fmaxf(fmaxf(s2[0], s2[2]), fmaxf(s3[0], s3[2])));
                const float f = __builtin_amdgcn_exp2f((mw - mc) * 1.4426950408889634f);
                pa = v4u{pk2(e[0] * f, e[1] * f), pk2(e[2] * f, e[3] * f), pk2(e[4] * f, e[5] * f), pk2(e[6] * f, e[7] * f)};
                float lc = 0.f;
                lc += s0[1] * __builtin_amdgcn_exp2f((s0[0] - mc) * 1.4426950408889634f); lc += s0[3] * __builtin_amdgcn_exp2f((s0[2] - mc) * 1.4426950408889634f);
                lc += s1[1] * __builtin_amdgcn_exp2f((s1[0] - mc) * 1.4426950408889634f); lc += s1[3] * __builtin_amdgcn_exp2f((s1[2] - mc) * 1.4426950408889634f);
                lc += s2[1] * __builtin_amdgcn_exp2f((s2[0] - mc) * 1.4426950408889634f); lc += s2[3] * __builtin_amdgcn_exp2f((s2[2] - mc) * 1.4426950408889634f);
                lc += s3[1] * __builtin_amdgcn_exp2f((s3[0] - mc) * 1.4426950408889634f); lc += s3[3] * __builtin_amdgcn_exp2f((s3[2] - mc) * 1.4426950408889634f);
                linv = 1.f / lc;
            }
            const float li0 = dpp<0x00>(linv), li1 = dpp<0x55>(linv), li2 = dpp<0xAA>(linv), li3 = dpp<0xFF>(linv);
            TS(4);
            v4u vo[8];
#pragma unroll
            for (int pr = 0; pr < 4; ++pr)
#pragma unroll
                for (int j = 0; j < 8; ++j) vo[j][pr] = __builtin_amdgcn_perm(rv[2 * pr + 1][j >> 1], rv[2 * pr][j >> 1], (j & 1) ? 0x07060302u : 0x05040100u);
            TS(5);
            v4u r0[4], r1[4], r2[4]; v2u cv;
            const int jm = xcd * 7 + idx;
            if (isM) {
                const size_t o = (size_t)r4 * 3584 + jm * 64 + wofs;
#pragma unroll
                for (int i = 0; i < 4; ++i) r0[i] = ldnt((const char*)wg + o + i * 3584);
#pragma unroll
                for (int i = 0; i < 4; ++i) r1[i] = ldnt((const char*)wu + o + i * 3584);
                const char* p = (const char*)wd + (size_t)(32 * jm + 4 * (l >> 3)) * 1024 + 128 * w + (l & 7) * 16;
#pragma unroll
                for (int i = 0; i < 4; ++i) r2[i] = ldnt(p + i * 1024);
                cv = *(const GAS v2u*)(xin + r4);
            } else if (isR) {
                const char* p = (const char*)wmod + (size_t)r4 * 6144 + 2048 + (xcd * 8 + idx - 7) * 64 + wofs;
#pragma unroll
                for (int i = 0; i < 4; ++i) r0[i] = ldnt(p + i * 6144);
                cv = *(const GAS v2u*)(cin + r4);
            }
            __builtin_amdgcn_sched_barrier(0);
            {
                v4f d[8];
#pragma unroll
                for (int j = 0; j < 8; ++j) d[j] = mfma32(pa, vo[j], v4f{0.f, 0.f, 0.f, 0.f});
                if (l < 16) {
#pragma unroll
                    for (int j = 0; j < 8; ++j) po[(w * 8 + j) * 16 + l] = d[j];
                }
            }
            bar();
            const u32 tag = epoch * 1664525u + 1013904223u;
            if (w < 2) {
                v4f o = po[tid];
#pragma unroll
                for (int i = 1; i < 8; ++i) o += po[i * 128 + tid];
                const u32 l0 = pk2(o[0] * li0, o[2] * li2);
                const u32 b1 = asu(o[1] * li1), b3 = asu(o[3] * li3);
                const u32 q0 = l0 & 0xffffu, q2 = l0 >> 16;
                const u32 m1 = (b1 & 0x7fffffffu) + 0x8000u, m3 = (b3 & 0x7fffffffu) + 0x8000u;
                const u32 h1 = (m1 > q0 ? m1 - q0 : 0u) & 0x7fff0000u, h3 = (m3 > q2 ? m3 - q2 : 0u) & 0x7fff0000u;
                st16(ws + OFF_XA + (g * 16 + s) * 2048 + tid * 16, v4u{(b1 & 0x80000000u) | h1 | q0, tag, (b3 & 0x80000000u) | h3 | q2, tag});
            }
            TS(6);
            if (isR) {
                v2u op[8];
                v2u a = {pk2(silu_bf(lo16(cv.x)), silu_bf(hi16(cv.x))), pk2(silu_bf(lo16(cv.y)), silu_bf(hi16(cv.y)))};
                perm4(op, r0);
                gemv32(red0, a, op, w, l);
                bar();
                TS(7);
            } else if (isM) {
                v2u og[8], ou[8], od[8];
                perm4(og, r0); perm4(ou, r1); perm4(od, r2);
                TS(7);
                v4u vm[3];
                { const char* base = ws + OFF_XM + r4 * 4; const char* const pp[3] = {base, base + 2048, base + 4096}; gather<3>(vm, pp, tag); }
                TS(8);
                float x2[4];
                {
                    v4u vw[4];
                    const char* base = ws + OFF_XW + ((r4 >> 5) * 8 + 2 * (l & 3)) * 256 + (r4 & 31) * 8;
                    const char* const pp[4] = {base, base + 16, base + 256, base + 272};
                    gather<4>(vw, pp, tag);
                    TS(9);
                    float a0 = asf(vw[0].x) + asf(vw[2].x), a1 = asf(vw[0].z) + asf(vw[2].z), a2 = asf(vw[1].x) + asf(vw[3].x), a3 = asf(vw[1].z) + asf(vw[3].z);
                    a0 += dpp<QP1>(a0); a1 += dpp<QP1>(a1); a2 += dpp<QP1>(a2); a3 += dpp<QP1>(a3);
                    a0 += dpp<QP2>(a0); a1 += dpp<QP2>(a1); a2 += dpp<QP2>(a2); a3 += dpp<QP2>(a3);
                    x2[0] = rbf(lo16(cv.x) + rbf(lo16(vm[0].x) * rbf(a0))); x2[1] = rbf(hi16(cv.x) + rbf(hi16(vm[0].x) * rbf(a1)));
                    x2[2] = rbf(lo16(cv.y) + rbf(lo16(vm[0].z) * rbf(a2))); x2[3] = rbf(hi16(cv.y) + rbf(hi16(vm[0].z) * rbf(a3)));
                }
                {
                    const float pv = asf(__builtin_amdgcn_readfirstlane(asu(x2[0])));
                    const float d0 = x2[0] - pv, d1 = x2[1] - pv, d2 = x2[2] - pv, d3 = x2[3] - pv;
                    float s1 = (d0 + d1) + (d2 + d3), s2 = (d0 * d0 + d1 * d1) + (d2 * d2 + d3 * d3);
                    s1 += dpp<ROR(4)>(s1); s2 += dpp<ROR(4)>(s2); s1 += dpp<ROR(8)>(s1); s2 += dpp<ROR(8)>(s2);
                    s1 = x32add(x16add(s1)); s2 = x32add(x16add(s2));
                    if (l == 0) *(v4f*)(lns + w * 4) = v4f{pv, s1, s2, 0.f};
                }
                bar();
                {
                    const v4f t = *(const v4f*)(lns + (l & 7) * 4);
                    float a = 64.f * t[0] + t[1];
                    a += dpp<QP1>(a); a += dpp<QP2>(a); a += dpp<ROR(4)>(a) ;
                    const float mean = a * (1.f / 512.f), ee = mean - t[0];
                    float v = t[2] - 2.f * ee * t[1] + 64.f * ee * ee;
                    v += dpp<QP1>(v); v += dpp<QP2>(v); v += dpp<ROR(4)>(v);
                    const float mb = rbf(mean);
                    const float dn = rbf(__builtin_sqrtf(rbf(rbf(fmaxf(v, 0.f) * (1.f / 512.f)) + 1e-5f)));
                    const float ri = 1.f / dn;
                    v2u a2;
                    a2.x = pk2(rbf(rbf(rbf(x2[0] - mb) * ri) * rbf(1.f + lo16(vm[2].x))) + lo16(vm[1].x), rbf(rbf(rbf(x2[1] - mb) * ri) * rbf(1.f + hi16(vm[2].x))) + hi16(vm[1].x));
                    a2.y = pk2(rbf(rbf(rbf(x2[2] - mb) * ri) * rbf(1.f + lo16(vm[2].z))) + lo16(vm[1].z), rbf(rbf(rbf(x2[3] - mb) * ri) * rbf(1.f + hi16(vm[2].z))) + hi16(vm[1].z));
                    gemv32(red0, a2, og, w, l);
                    gemv32(red1, a2, ou, w, l);
                }
                bar();
                TS(10);
                {
                    const float* rr = (l < 32) ? red0 : red1;
                    const v4f t = *(const v4f*)(rr + (4 * w + ((l >> 3) & 3)) * 36 + (l & 7) * 4);
                    float a = (t[0] + t[1]) + (t[2] + t[3]);
                    a += dpp<QP1>(a); a += dpp<QP2>(a); a += dpp<0x141>(a);
                    auto r = __builtin_amdgcn_permlane32_swap(asu(a), asu(a), false, false);
                    const float gg = rbf(asf(r[0])), uu = rbf(asf(r[1]));
                    const float tt = 0.7978845608028654f * (gg + 0.044715f * gg * gg * gg);
                    const float th = 1.f - 2.f * __builtin_amdgcn_rcpf(1.f + __builtin_amdgcn_exp2f(tt * 2.885390081777927f));
                    const float hv = rbf(0.5f * gg * (1.f + th)) * uu;
                    const u32 hp = pk2(hv, dpp<ROR(8)>(hv));
                    if ((l & 47) == 0) hhs[2 * w + (l >> 4)] = hp;
                }
                bar();
                {
                    const v2u a = *(const v2u*)(hhs + 2 * (2 * (l >> 4) + (l & 1)));
                    float y[8];
#pragma unroll
                    for (int j = 0; j < 8; ++j) { const v4f d = mfma16(a, od[j]); y[j] = d[0] + dpp<ROR(8)>(d[1]); }
                    const int q = l >> 4;
                    const float y0 = q == 0 ? y[0] : q == 1 ? y[2] : q == 2 ? y[4] : y[6];
                    const float y1 = q == 0 ? y[1] : q == 1 ? y[3] : q == 2 ? y[5] : y[7];
                    if ((l & 8) == 0) st16(ws + OFF_XD + jm * 4096 + (64 * w + 8 * (l & 7) + 2 * q) * 8, v4u{asu(y0), tag, asu(y1), tag});
                }
                TS(11);
            }
        } else {
            const u32 tag = epoch * 1664525u + 1013904223u;
            asm volatile("" :: "v"(pfd), "v"(tag));
            {
                v4u v2[2]; const char* base = ws + OFF_XQ + g * 2048 + l * 16; const char* const pp[2] = {base, base + 1024};
                gather<2>(v2, pp, tag);
                *(v2u*)(qs + 2 * l) = v2u{v2[0].x, v2[0].z};
                *(v2u*)(qs + 128 + 2 * l) = v2u{v2[1].x, v2[1].z};
            }
            TS(2);
            bar();
            bar();
            {
                const v4f* q = (const v4f*)(st + (l & 3) * 16);
                const v4f s0 = q[0], s1 = q[1], s2 = q[2], s3 = q[3];
                const float mc = fmaxf(fmaxf(fmaxf(s0[0], s0[2]), fmaxf(s1[0], s1[2])), fmaxf(fmaxf(s2[0], s2[2]), fmaxf(s3[0], s3[2])));
                float lc = 0.f;
                lc += s0[1] * __builtin_amdgcn_exp2f((s0[0] - mc) * 1.4426950408889634f); lc += s0[3] * __builtin_amdgcn_exp2f((s0[2] - mc) * 1.4426950408889634f);
                lc += s1[1] * __builtin_amdgcn_exp2f((s1[0] - mc) * 1.4426950408889634f); lc += s1[3] * __builtin_amdgcn_exp2f((s1[2] - mc) * 1.4426950408889634f);
                lc += s2[1] * __builtin_amdgcn_exp2f((s2[0] - mc) * 1.4426950408889634f); lc += s2[3] * __builtin_amdgcn_exp2f((s2[2] - mc) * 1.4426950408889634f);
                lc += s3[1] * __builtin_amdgcn_exp2f((s3[0] - mc) * 1.4426950408889634f); lc += s3[3] * __builtin_amdgcn_exp2f((s3[2] - mc) * 1.4426950408889634f);
                const float z = mc * 1.4426950408889634f + __builtin_amdgcn_logf(lc);
                const float z1 = dpp<QP1>(z);
                if (l == 0 || l == 2) st16(ws + OFF_XS + (g * 16 + s) * 128 + l * 8, v4u{asu(z), tag, asu(z1), tag});
            }
            TS(4);
            bar();
            if (isR) { bar(); pub_bf16(ws + OFF_XM + (xcd * 8 + idx - 7) * 128, red32(red0, l), tag, l); TS(7); }
            else if (isM) { bar(); bar(); bar(); }
        }
    }
    TSDUMP();
}

extern "C" void kernel_launch(void* const* d_in, const int* in_sizes, int n_in, void* d_out, int out_size, void* d_ws, size_t ws_size, hipStream_t stream) {
    fused_kernel<<<256, 576, 0, stream>>>((const u16*)d_in[0], (const u16*)d_in[1], (const u16*)d_in[2], (const u16*)d_in[3], (const u16*)d_in[4],
        (const u16*)d_in[6], (const u16*)d_in[7], (const u16*)d_in[8], (const u16*)d_in[9], (const u16*)d_in[10], (u16*)d_out, (char*)d_ws);
}
```

```cpp
#include <hip/hip_runtime.h>
#include <stdint.h>

typedef unsigned u32;
typedef unsigned short u16;
typedef u32 v4u __attribute__((ext_vector_type(4)));
typedef u32 v2u __attribute__((ext_vector_type(2)));
typedef float v4f __attribute__((ext_vector_type(4)));
typedef float v2f __attribute__((ext_vector_type(2)));
typedef short v4s __attribute__((ext_vector_type(4)));
typedef __bf16 v8bf __attribute__((ext_vector_type(8)));
typedef __bf16 v2bf __attribute__((ext_vector_type(2)));
#define GAS __attribute__((address_space(1)))

#define DLY_A 0
#define DLY_WO 0

constexpr u32 OFF_EP = 0;
constexpr u32 OFF_XH = 4096;
constexpr u32 OFF_XM = 8192;
constexpr u32 OFF_XQ = 16384;
constexpr u32 OFF_XS = 32768;
constexpr u32 OFF_XW = 49152;
constexpr u32 OFF_XA = 131072;
constexpr u32 OFF_XD = 393216;
constexpr u32 OFF_DBG = 1u << 20;
constexpr int SPIN_MAX = 1 << 16;

__device__ __forceinline__ float asf(u32 v) { return __uint_as_float(v); }
__device__ __forceinline__ u32 asu(float v) { return __float_as_uint(v); }
__device__ __forceinline__ float lo16(u32 v) { return asf(v << 16); }
__device__ __forceinline__ float hi16(u32 v) { return asf(v & 0xffff0000u); }
__device__ __forceinline__ u32 pk2(float a, float b) { v2f t = {a, b}; v2bf r = __builtin_convertvector(t, v2bf); return __builtin_bit_cast(u32, r); }
__device__ __forceinline__ float rbf(float a) { return lo16(pk2(a, a) & 0xffffu); }
__device__ __forceinline__ v4u ldnt(const void* p) { return __builtin_nontemporal_load((const GAS v4u*)p); }
__device__ __forceinline__ void st16(void* p, v4u v) { asm volatile("global_store_dwordx4 %0, %1, off sc1\n s_nop 1" : : "v"(p), "v"(v) : "memory"); }
__device__ __forceinline__ v4u ld16(const void* p) { v4u v; asm volatile("global_load_dwordx4 %0, %1, off sc1" : "=v"(v) : "v"(p) : "memory"); return v; }
__device__ __forceinline__ void bar() { asm volatile("s_waitcnt lgkmcnt(0)\n s_barrier" ::: "memory"); }
template <int C> __device__ __forceinline__ float dpp(float x) { return asf(__builtin_amdgcn_update_dpp(0, asu(x), C, 0xf, 0xf, true)); }
#define QP1 0xB1
#define QP2 0x4E
#define ROR(n) (0x120 + (n))
__device__ __forceinline__ float x16add(float x) { auto r = __builtin_amdgcn_permlane16_swap(asu(x), asu(x), false, false); return asf(r[0]) + asf(r[1]); }
__device__ __forceinline__ float x32add(float x) { auto r = __builtin_amdgcn_permlane32_swap(asu(x), asu(x), false, false); return asf(r[0]) + asf(r[1]); }
__device__ __forceinline__ float x16max(float x) { auto r = __builtin_amdgcn_permlane16_swap(asu(x), asu(x), false, false); return fmaxf(asf(r[0]), asf(r[1])); }
__device__ __forceinline__ float x32max(float x) { auto r = __builtin_amdgcn_permlane32_swap(asu(x), asu(x), false, false); return fmaxf(asf(r[0]), asf(r[1])); }

template <int N> __device__ __forceinline__ void gather(v4u (&v)[N], const char* const (&p)[N], u32 tag) {
    for (int spin = 0; spin < SPIN_MAX; ++spin) {
#pragma unroll
        for (int i = 0; i < N; ++i) v[i] = ld16(p[i]);
        asm volatile("s_waitcnt vmcnt(0)" ::: "memory");
        u32 bad = 0;
#pragma unroll
        for (int i = 0; i < N; ++i) { asm volatile("" : "+v"(v[i])); bad |= (v[i].y ^ tag) | (v[i].w ^ tag); }
        if (__all(bad == 0)) break;
    }
}
__device__ __forceinline__ void perm4(v2u (&op)[8], const v4u (&r)[4]) {
#pragma unroll
    for (int j = 0; j < 8; ++j) {
        const u32 sel = (j & 1) ? 0x07060302u : 0x05040100u;
        op[j].x = __builtin_amdgcn_perm(r[1][j >> 1], r[0][j >> 1], sel);
        op[j].y = __builtin_amdgcn_perm(r[3][j >> 1], r[2][j >> 1], sel);
    }
}
__device__ __forceinline__ v4f mfma16(v2u a, v2u b) {
    return __builtin_amdgcn_mfma_f32_16x16x16bf16_1k(__builtin_bit_cast(v4s, a), __builtin_bit_cast(v4s, b), v4f{0.f, 0.f, 0.f, 0.f}, 0, 0, 0);
}
__device__ __forceinline__ v4f mfma32(v4u a, v4u b, v4f c) {
    return __builtin_amdgcn_mfma_f32_16x16x32_bf16(__builtin_bit_cast(v8bf, a), __builtin_bit_cast(v8bf, b), c, 0, 0, 0);
}
__device__ __forceinline__ void gemv32(float* red, v2u a, const v2u (&op)[8], int w, int l) {
    v4f d[8];
#pragma unroll
    for (int j = 0; j < 8; ++j) d[j] = mfma16(a, op[j]);
    if (((l >> 2) & 3) == (l >> 4)) {
        float* q = red + (8 * (l & 3)) * 36 + 4 * w + (l >> 4);
#pragma unroll
        for (int j = 0; j < 8; ++j) q[j * 36] = d[j][0];
    }
}
__device__ __forceinline__ float red32(const float* red, int l) {
    const v4f* q = (const v4f*)(red + (l & 31) * 36 + 16 * (l >> 5));
    v4f s = (q[0] + q[1]) + (q[2] + q[3]);
    return x32add((s[0] + s[1]) + (s[2] + s[3]));
}
__device__ __forceinline__ void pub_bf16(char* line, float r, u32 tag, int l) {
    float r1 = dpp<QP1>(r);
    u32 p = pk2(r, r1);
    u32 p2 = __builtin_amdgcn_update_dpp(0, p, QP2, 0xf, 0xf, true);
    if (l < 32 && (l & 3) == 0) st16(line + l * 4, v4u{p, tag, p2, tag});
}
__device__ __forceinline__ float silu_bf(float v) { return v * __builtin_amdgcn_rcpf(1.f + __expf(-v)); }

#define TS(i)
#define TSDUMP()

__global__ void __launch_bounds__(576) fused_kernel(const u16* __restrict__ xin, const u16* __restrict__ kc, const u16* __restrict__ vc,
        const u16* __restrict__ wq, const u16* __restrict__ wo, const u16* __restrict__ wg, const u16* __restrict__ wu,
        const u16* __restrict__ wd, const u16* __restrict__ cin, const u16* __restrict__ wmod, u16* __restrict__ out, char* __restrict__ ws) {
    __shared__ __attribute__((aligned(16))) float red0[32 * 36];
    __shared__ __attribute__((aligned(16))) float red1[32 * 36];
    __shared__ __attribute__((aligned(16))) float red2[32 * 36];
    __shared__ __attribute__((aligned(16))) v4f po[8 * 128];
    __shared__ __attribute__((aligned(16))) u32 qs[256];
    __shared__ __attribute__((aligned(16))) float st[64];
    __shared__ __attribute__((aligned(16))) float wt[64];
    __shared__ __attribute__((aligned(16))) float lns[32];
    __shared__ __attribute__((aligned(16))) u32 hhs[16];
    const int b = blockIdx.x, tid = threadIdx.x, l = tid & 63;
    const int w = __builtin_amdgcn_readfirstlane(tid >> 6);
    const int xcd = b & 7, idx = b >> 3;
    const u32 tstart = (u32)__builtin_amdgcn_s_memrealtime();
    TS(0);
    const u32 epoch = __hip_atomic_load((const GAS u32*)(ws + OFF_EP), __ATOMIC_RELAXED, __HIP_MEMORY_SCOPE_AGENT);
    const int r4 = 64 * w + (l & 60);
    const u32 wofs = (u32)(l & 3) * 16;

    if (idx >= 16) {
        const int c = idx - 16, g = (xcd + 1) & 7;
        if (w < 8) {
            v4u rm[4], rq[4]; v2u cv;
            const bool ismod = c < 4;
            if (ismod) {
                cv = *(const GAS v2u*)(cin + r4);
                const char* p = (const char*)wmod + (size_t)r4 * 6144 + (xcd * 4 + c) * 64 + wofs;
#pragma unroll
                for (int i = 0; i < 4; ++i) rm[i] = ldnt(p + i * 6144);
            }
            const v4u xa = *(const GAS v4u*)(xin + 8 * l);
            const v2u xo = *(const GAS v2u*)(xin + r4);
            {
                const char* p = (const char*)wq + (size_t)r4 * 8192 + (g * 512 + c * 32) * 2 + wofs;
#pragma unroll
                for (int i = 0; i < 4; ++i) rq[i] = ldnt(p + i * 8192);
            }
            __builtin_amdgcn_sched_barrier(0);
            v2u op[8];
            if (ismod) {
                v2u a = {pk2(silu_bf(lo16(cv.x)), silu_bf(hi16(cv.x))), pk2(silu_bf(lo16(cv.y)), silu_bf(hi16(cv.y)))};
                perm4(op, rm);
                gemv32(red0, a, op, w, l);
                TS(1);
                bar();
            }
            float xv[8] = {lo16(xa.x), hi16(xa.x), lo16(xa.y), hi16(xa.y), lo16(xa.z), hi16(xa.z), lo16(xa.w), hi16(xa.w)};
            float s = ((xv[0] + xv[1]) + (xv[2] + xv[3])) + ((xv[4] + xv[5]) + (xv[6] + xv[7]));
            s += dpp<QP1>(s); s += dpp<QP2>(s); s += dpp<ROR(4)>(s); s += dpp<ROR(8)>(s); s = x32add(x16add(s));
            const float mean = s * (1.f / 512.f);
            float v = 0.f;
#pragma unroll
            for (int i = 0; i < 8; ++i) { float d = xv[i] - mean; v += d * d; }
            v += dpp<QP1>(v); v += dpp<QP2>(v); v += dpp<ROR(4)>(v); v += dpp<ROR(8)>(v); v = x32add(x16add(v));
            const float mb = rbf(mean);
            const float dn = rbf(__builtin_sqrtf(rbf(rbf(v * (1.f / 512.f)) + 1e-5f)));
            float n[4] = {rbf(rbf(lo16(xo.x) - mb) / dn), rbf(rbf(hi16(xo.x) - mb) / dn), rbf(rbf(lo16(xo.y) - mb) / dn), rbf(rbf(hi16(xo.y) - mb) / dn)};
            perm4(op, rq);
            const u32 tag = epoch * 1664525u + 1013904223u;
            TS(2);
            {
                v4u v2[2]; const char* const pp[2] = {ws + OFF_XH + r4 * 4, ws + OFF_XH + 2048 + r4 * 4};
                gather<2>(v2, pp, tag);
                TS(3);
                v2u a;
                a.x = pk2(rbf(n[0] * rbf(1.f + lo16(v2[1].x))) + lo16(v2[0].x), rbf(n[1] * rbf(1.f + hi16(v2[1].x))) + hi16(v2[0].x));
                a.y = pk2(rbf(n[2] * rbf(1.f + lo16(v2[1].z))) + lo16(v2[0].z), rbf(n[3] * rbf(1.f + hi16(v2[1].z))) + hi16(v2[0].z));
                gemv32(red1, a, op, w, l);
            }
            bar();
            TS(4);
            if (DLY_WO) __builtin_amdgcn_s_sleep(DLY_WO);
            {
                const char* p = (const char*)wo + (size_t)(g * 512 + 8 * (l >> 2) + w) * 1024 + c * 64 + wofs;
#pragma unroll
                for (int i = 0; i < 4; ++i) rq[i] = ldnt(p + i * 131072);
            }
            __builtin_amdgcn_sched_barrier(0);
            bar();
            TS(5);
            v4f wv[4];
#pragma unroll
            for (int i = 0; i < 4; ++i) wv[i] = *(const v4f*)(wt + (4 * (l & 3) + i) * 4);
            perm4(op, rq);
            TS(6);
            {
                v4u va[4];
                const char* base = ws + OFF_XA + (g * 16 + 4 * (l & 3)) * 2048 + (16 * w + (l >> 2)) * 16;
                const char* const pp[4] = {base, base + 2048, base + 4096, base + 6144};
                gather<4>(va, pp, tag);
                TS(7);
                v2f a01 = {0.f, 0.f}, a23 = {0.f, 0.f};
#pragma unroll
                for (int i = 0; i < 4; ++i) {
                    a01 += v2f{asf(va[i].x), lo16(va[i].x)} * v2f{wv[i][0], wv[i][1]};
                    a23 += v2f{asf(va[i].z), lo16(va[i].z)} * v2f{wv[i][2], wv[i][3]};
                }
                float o1 = a01[0], o0 = a01[1], o3 = a23[0], o2 = a23[1];
                o0 += dpp<QP1>(o0); o1 += dpp<QP1>(o1); o2 += dpp<QP1>(o2); o3 += dpp<QP1>(o3);
                o0 += dpp<QP2>(o0); o1 += dpp<QP2>(o1); o2 += dpp<QP2>(o2); o3 += dpp<QP2>(o3);
                gemv32(red2, v2u{pk2(o0, o1), pk2(o2, o3)}, op, w, l);
            }
            bar();
            TS(8);
        } else {
            const u32 tag = epoch * 1664525u + 1013904223u;
            if (l == 0) st16(ws + (2u << 20) + b * 16, v4u{tstart, tag, tstart, tag});
            if (c < 4) { bar(); pub_bf16(ws + OFF_XH + (xcd * 4 + c) * 128, red32(red0, l), tag, l); TS(1); }
            bar();
            pub_bf16(ws + OFF_XQ + (g * 16 + c) * 128, red32(red1, l), tag, l);
            TS(4);
            {
                v4u vs[1]; const char* const pp[1] = {ws + OFF_XS + (g * 16 + (l & 15)) * 128 + ((l >> 4) & 1) * 16};
                gather<1>(vs, pp, tag);
                TS(5);
                float z0 = asf(vs[0].x), z1 = asf(vs[0].z), m0 = z0, m1 = z1;
                m0 = fmaxf(m0, dpp<QP1>(m0)); m1 = fmaxf(m1, dpp<QP1>(m1)); m0 = fmaxf(m0, dpp<QP2>(m0)); m1 = fmaxf(m1, dpp<QP2>(m1));
                m0 = fmaxf(m0, dpp<ROR(4)>(m0)); m1 = fmaxf(m1, dpp<ROR(4)>(m1)); m0 = fmaxf(m0, dpp<ROR(8)>(m0)); m1 = fmaxf(m1, dpp<ROR(8)>(m1));
                float e0 = __builtin_amdgcn_exp2f(z0 - m0), e1 = __builtin_amdgcn_exp2f(z1 - m1), s0 = e0, s1 = e1;
                s0 += dpp<QP1>(s0); s1 += dpp<QP1>(s1); s0 += dpp<QP2>(s0); s1 += dpp<QP2>(s1);
                s0 += dpp<ROR(4)>(s0); s1 += dpp<ROR(4)>(s1); s0 += dpp<ROR(8)>(s0); s1 += dpp<ROR(8)>(s1);
                if (l < 32) *(v2f*)(wt + (l & 15) * 4 + (l >> 4) * 2) = v2f{e1 / s1, e0 / s0};
            }
            bar();
            bar();
            {
                float r = red32(red2, l), r1 = dpp<QP1>(r);
                if (l < 32 && (l & 1) == 0) st16(ws + OFF_XW + (c * 8 + g) * 256 + l * 8, v4u{asu(r), tag, asu(r1), tag});
            }
            TS(8);
            if (c < 4) {
                const int col = (xcd * 4 + c) * 16 + 2 * (l & 7), pg = l >> 3;
                const u32 xr = *(const GAS u32*)(xin + col);
                v4u v3[3];
                const char* const pp[3] = {ws + OFF_XW + ((col >> 5) * 8 + pg) * 256 + (col & 31) * 8, ws + OFF_XM + (col & ~3) * 4, ws + OFF_XM + 6144 + (col & ~3) * 4};
                gather<3>(v3, pp, tag);
                float a0 = asf(v3[0].x), a1 = asf(v3[0].z);
                a0 += dpp<ROR(8)>(a0); a1 += dpp<ROR(8)>(a1); a0 = x32add(x16add(a0)); a1 = x32add(x16add(a1));
                const u32 ga = (l & 1) ? v3[1].z : v3[1].x, gb = (l & 1) ? v3[2].z : v3[2].x;
                const float x20 = rbf(lo16(xr) + rbf(lo16(ga) * rbf(a0))), x21 = rbf(hi16(xr) + rbf(hi16(ga) * rbf(a1)));
                TS(9);
                v4u vd[7];
                const char* base = ws + OFF_XD + pg * 4096 + col * 8;
                const char* const pd[7] = {base, base + 32768, base + 65536, base + 98304, base + 131072, base + 163840, base + 196608};
                gather<7>(vd, pd, tag);
                TS(10);
                float y0 = 0.f, y1 = 0.f;
#pragma unroll
                for (int i = 0; i < 7; ++i) { y0 += asf(vd[i].x); y1 += asf(vd[i].z); }
                y0 += dpp<ROR(8)>(y0); y1 += dpp<ROR(8)>(y1); y0 = x32add(x16add(y0)); y1 = x32add(x16add(y1));
                const u32 o = pk2(x20 + rbf(lo16(gb) * rbf(y0)), x21 + rbf(hi16(gb) * rbf(y1)));
                if (l < 8) *(GAS u32*)(out + col) = o;
                if (b == 128 && l == 0) __hip_atomic_store((GAS u32*)(ws + OFF_EP), tag, __ATOMIC_RELAXED, __HIP_MEMORY_SCOPE_AGENT);
                TS(11);
                if (b == 128) {
                    v4u vt[4]; const char* tb = ws + (2u << 20) + l * 16; const char* const pt[4] = {tb, tb + 1024, tb + 2048, tb + 3072};
                    gather<4>(vt, pt, tag);
                    int mn = 1 << 30, mx = -(1 << 30);
                    for (int i = 0; i < 4; ++i) { int d = (int)(vt[i].x - tstart); mn = min(mn, d); mx = max(mx, d); }
                    for (int o2 = 32; o2 > 0; o2 >>= 1) { mn = min(mn, __shfl_xor(mn, o2)); mx = max(mx, __shfl_xor(mx, o2)); }
                    const u32 now = (u32)__builtin_amdgcn_s_memrealtime();
                    const u32 dly = __builtin_amdgcn_readfirstlane((mx - mn) & 0);
                    while ((u32)__builtin_amdgcn_s_memrealtime() - now < dly && dly < 20000) {}
                }
            }
        }
    } else {
        const int g = xcd, s = idx;
        const bool isM = idx < 7, isR = idx >= 7 && idx < 15;
        if (w < 8) {
            if (DLY_A) __builtin_amdgcn_s_sleep(DLY_A);
            v4u rk[8], rv[8];
            const size_t kb = ((size_t)g * 65536 + 61440 + 256 * s + 32 * w) * 256;
            {
                const char* p = (const char*)kc + kb + (l & 15) * 256 + (l >> 4) * 16;
#pragma unroll
                for (int i = 0; i < 8; ++i) rk[i] = ldnt(p + (i >> 2) * 4096 + (i & 3) * 64);
                const char* q = (const char*)vc + kb + (l >> 4) * 1024 + (l & 15) * 16;
#pragma unroll
                for (int i = 0; i < 8; ++i) rv[i] = ldnt(q + (i >> 2) * 4096 + (i & 3) * 256);
            }
            __builtin_amdgcn_sched_barrier(0);
            TS(1);
            bar();
            TS(2);
            v4f sa = {0.f, 0.f, 0.f, 0.f}, sb = sa;
#pragma unroll
            for (int t = 0; t < 4; ++t) {
                const v4u qv = *(const v4u*)(qs + (l & 3) * 64 + (4 * t + (l >> 4)) * 4);
                sa = mfma32(rk[t], qv, sa); sb = mfma32(rk[4 + t], qv, sb);
            }
            float e[8], mw;
#pragma unroll
            for (int i = 0; i < 4; ++i) { e[i] = rbf(rbf(sa[i]) * 0.08838834764831845f); e[4 + i] = rbf(rbf(sb[i]) * 0.08838834764831845f); }
            mw = fmaxf(fmaxf(fmaxf(e[0], e[1]), fmaxf(e[2], e[3])), fmaxf(fmaxf(e[4], e[5]), fmaxf(e[6], e[7])));
            mw = x32max(x16max(mw));
            float lw = 0.f;
#pragma unroll
            for (int i = 0; i < 8; ++i) { e[i] = __builtin_amdgcn_exp2f((e[i] - mw) * 1.4426950408889634f); lw += e[i]; }
            lw = x32add(x16add(lw));
            if (l < 4) *(v2f*)(st + l * 16 + w * 2) = v2f{mw, lw};
            TS(3);
            bar();
            v4u pa; float linv;
            {
                const v4f* q = (const v4f*)(st + (l & 3) * 16);
                const v4f s0 = q[0], s1 = q[1], s2 = q[2], s3 = q[3];
                const float mc = fmaxf(fmaxf(fmaxf(s0[0], s0[2]), fmaxf(s1[0], s1[2])), fmaxf(fmaxf(s2[0], s2[2]), fmaxf(s3[0], s3[2])));
                const float f = __builtin_amdgcn_exp2f((mw - mc) * 1.4426950408889634f);
                pa = v4u{pk2(e[0] * f, e[1] * f), pk2(e[2] * f, e[3] * f), pk2(e[4] * f, e[5] * f), pk2(e[6] * f, e[7] * f)};
                float lc = 0.f;
                lc += s0[1] * __builtin_amdgcn_exp2f((s0[0] - mc) * 1.4426950408889634f); lc += s0[3] * __builtin_amdgcn_exp2f((s0[2] - mc) * 1.4426950408889634f);
                lc += s1[1] * __builtin_amdgcn_exp2f((s1[0] - mc) * 1.4426950408889634f); lc += s1[3] * __builtin_amdgcn_exp2f((s1[2] - mc) * 1.4426950408889634f);
                lc += s2[1] * __builtin_amdgcn_exp2f((s2[0] - mc) * 1.4426950408889634f); lc += s2[3] * __builtin_amdgcn_exp2f((s2[2] - mc) * 1.4426950408889634f);
                lc += s3[1] * __builtin_amdgcn_exp2f((s3[0] - mc) * 1.4426950408889634f); lc += s3[3] * __builtin_amdgcn_exp2f((s3[2] - mc) * 1.4426950408889634f);
                linv = 1.f / lc;
            }
            const float li0 = dpp<0x00>(linv), li1 = dpp<0x55>(linv), li2 = dpp<0xAA>(linv), li3 = dpp<0xFF>(linv);
            TS(4);
            v4u vo[8];
#pragma unroll
            for (int pr = 0; pr < 4; ++pr)
#pragma unroll
                for (int j = 0; j < 8; ++j) vo[j][pr] = __builtin_amdgcn_perm(rv[2 * pr + 1][j >> 1], rv[2 * pr][j >> 1], (j & 1) ? 0x07060302u : 0x05040100u);
            TS(5);
            v4u r0[4], r1[4], r2[4]; v2u cv;
            const int jm = xcd * 7 + idx;
            if (isM) {
                const size_t o = (size_t)r4 * 3584 + jm * 64 + wofs;
#pragma unroll
                for (int i = 0; i < 4; ++i) r0[i] = ldnt((const char*)wg + o + i * 3584);
#pragma unroll
                for (int i = 0; i < 4; ++i) r1[i] = ldnt((const char*)wu + o + i * 3584);
                const char* p = (const char*)wd + (size_t)(32 * jm + 4 * (l >> 3)) * 1024 + 128 * w + (l & 7) * 16;
#pragma unroll
                for (int i = 0; i < 4; ++i) r2[i] = ldnt(p + i * 1024);
                cv = *(const GAS v2u*)(xin + r4);
            } else if (isR) {
                const char* p = (const char*)wmod + (size_t)r4 * 6144 + 2048 + (xcd * 8 + idx - 7) * 64 + wofs;
#pragma unroll
                for (int i = 0; i < 4; ++i) r0[i] = ldnt(p + i * 6144);
                cv = *(const GAS v2u*)(cin + r4);
            }
            __builtin_amdgcn_sched_barrier(0);
            {
                v4f d[8];
#pragma unroll
                for (int j = 0; j < 8; ++j) d[j] = mfma32(pa, vo[j], v4f{0.f, 0.f, 0.f, 0.f});
                if (l < 16) {
#pragma unroll
                    for (int j = 0; j < 8; ++j) po[(w * 8 + j) * 16 + l] = d[j];
                }
            }
            bar();
            const u32 tag = epoch * 1664525u + 1013904223u;
            if (w < 2) {
                v4f o = po[tid];
#pragma unroll
                for (int i = 1; i < 8; ++i) o += po[i * 128 + tid];
                const u32 l0 = pk2(o[0] * li0, o[2] * li2);
                const u32 b1 = asu(o[1] * li1), b3 = asu(o[3] * li3);
                const u32 q0 = l0 & 0xffffu, q2 = l0 >> 16;
                const u32 m1 = (b1 & 0x7fffffffu) + 0x8000u, m3 = (b3 & 0x7fffffffu) + 0x8000u;
                const u32 h1 = (m1 > q0 ? m1 - q0 : 0u) & 0x7fff0000u, h3 = (m3 > q2 ? m3 - q2 : 0u) & 0x7fff0000u;
                st16(ws + OFF_XA + (g * 16 + s) * 2048 + tid * 16, v4u{(b1 & 0x80000000u) | h1 | q0, tag, (b3 & 0x80000000u) | h3 | q2, tag});
            }
            TS(6);
            if (isR) {
                v2u op[8];
                v2u a = {pk2(silu_bf(lo16(cv.x)), silu_bf(hi16(cv.x))), pk2(silu_bf(lo16(cv.y)), silu_bf(hi16(cv.y)))};
                perm4(op, r0);
                gemv32(red0, a, op, w, l);
                bar();
                TS(7);
            } else if (isM) {
                v2u og[8], ou[8], od[8];
                perm4(og, r0); perm4(ou, r1); perm4(od, r2);
                TS(7);
                v4u vm[3];
                { const char* base = ws + OFF_XM + r4 * 4; const char* const pp[3] = {base, base + 2048, base + 4096}; gather<3>(vm, pp, tag); }
                TS(8);
                float x2[4];
                {
                    v4u vw[4];
                    const char* base = ws + OFF_XW + ((r4 >> 5) * 8 + 2 * (l & 3)) * 256 + (r4 & 31) * 8;
                    const char* const pp[4] = {base, base + 16, base + 256, base + 272};
                    gather<4>(vw, pp, tag);
                    TS(9);
                    float a0 = asf(vw[0].x) + asf(vw[2].x), a1 = asf(vw[0].z) + asf(vw[2].z), a2 = asf(vw[1].x) + asf(vw[3].x), a3 = asf(vw[1].z) + asf(vw[3].z);
                    a0 += dpp<QP1>(a0); a1 += dpp<QP1>(a1); a2 += dpp<QP1>(a2); a3 += dpp<QP1>(a3);
                    a0 += dpp<QP2>(a0); a1 += dpp<QP2>(a1); a2 += dpp<QP2>(a2); a3 += dpp<QP2>(a3);
                    x2[0] = rbf(lo16(cv.x) + rbf(lo16(vm[0].x) * rbf(a0))); x2[1] = rbf(hi16(cv.x) + rbf(hi16(vm[0].x) * rbf(a1)));
                    x2[2] = rbf(lo16(cv.y) + rbf(lo16(vm[0].z) * rbf(a2))); x2[3] = rbf(hi16(cv.y) + rbf(hi16(vm[0].z) * rbf(a3)));
                }
                {
                    const float pv = asf(__builtin_amdgcn_readfirstlane(asu(x2[0])));
                    const float d0 = x2[0] - pv, d1 = x2[1] - pv, d2 = x2[2] - pv, d3 = x2[3] - pv;
                    float s1 = (d0 + d1) + (d2 + d3), s2 = (d0 * d0 + d1 * d1) + (d2 * d2 + d3 * d3);
                    s1 += dpp<ROR(4)>(s1); s2 += dpp<ROR(4)>(s2); s1 += dpp<ROR(8)>(s1); s2 += dpp<ROR(8)>(s2);
                    s1 = x32add(x16add(s1)); s2 = x32add(x16add(s2));
                    if (l == 0) *(v4f*)(lns + w * 4) = v4f{pv, s1, s2, 0.f};
                }
                bar();
                {
                    const v4f t = *(const v4f*)(lns + (l & 7) * 4);
                    float a = 64.f * t[0] + t[1];
                    a += dpp<QP1>(a); a += dpp<QP2>(a); a += dpp<ROR(4)>(a) ;
                    const float mean = a * (1.f / 512.f), ee = mean - t[0];
                    float v = t[2] - 2.f * ee * t[1] + 64.f * ee * ee;
                    v += dpp<QP1>(v); v += dpp<QP2>(v); v += dpp<ROR(4)>(v);
                    const float mb = rbf(mean);
                    const float dn = rbf(__builtin_sqrtf(rbf(rbf(fmaxf(v, 0.f) * (1.f / 512.f)) + 1e-5f)));
                    const float ri = 1.f / dn;
                    v2u a2;
                    a2.x = pk2(rbf(rbf(rbf(x2[0] - mb) * ri) * rbf(1.f + lo16(vm[2].x))) + lo16(vm[1].x), rbf(rbf(rbf(x2[1] - mb) * ri) * rbf(1.f + hi16(vm[2].x))) + hi16(vm[1].x));
                    a2.y = pk2(rbf(rbf(rbf(x2[2] - mb) * ri) * rbf(1.f + lo16(vm[2].z))) + lo16(vm[1].z), rbf(rbf(rbf(x2[3] - mb) * ri) * rbf(1.f + hi16(vm[2].z))) + hi16(vm[1].z));
                    gemv32(red0, a2, og, w, l);
                    gemv32(red1, a2, ou, w, l);
                }
                bar();
                TS(10);
                {
                    const float* rr = (l < 32) ? red0 : red1;
                    const v4f t = *(const v4f*)(rr + (4 * w + ((l >> 3) & 3)) * 36 + (l & 7) * 4);
                    float a = (t[0] + t[1]) + (t[2] + t[3]);
                    a += dpp<QP1>(a); a += dpp<QP2>(a); a += dpp<0x141>(a);
                    auto r = __builtin_amdgcn_permlane32_swap(asu(a), asu(a), false, false);
                    const float gg = rbf(asf(r[0])), uu = rbf(asf(r[1]));
                    const float tt = 0.7978845608028654f * (gg + 0.044715f * gg * gg * gg);
                    const float th = 1.f - 2.f * __builtin_amdgcn_rcpf(1.f + __builtin_amdgcn_exp2f(tt * 2.885390081777927f));
                    const float hv = rbf(0.5f * gg * (1.f + th)) * uu;
                    const u32 hp = pk2(hv, dpp<ROR(8)>(hv));
                    if ((l & 47) == 0) hhs[2 * w + (l >> 4)] = hp;
                }
                bar();
                {
                    const v2u a = *(const v2u*)(hhs + 2 * (2 * (l >> 4) + (l & 1)));
                    float y[8];
#pragma unroll
                    for (int j = 0; j < 8; ++j) { const v4f d = mfma16(a, od[j]); y[j] = d[0] + dpp<ROR(8)>(d[1]); }
                    const int q = l >> 4;
                    const float y0 = q == 0 ? y[0] : q == 1 ? y[2] : q == 2 ? y[4] : y[6];
                    const float y1 = q == 0 ? y[1] : q == 1 ? y[3] : q == 2 ? y[5] : y[7];
                    if ((l & 8) == 0) st16(ws + OFF_XD + jm * 4096 + (64 * w + 8 * (l & 7) + 2 * q) * 8, v4u{asu(y0), tag, asu(y1), tag});
                }
                TS(11);
            }
        } else {
            const u32 tag = epoch * 1664525u + 1013904223u;
            if (l == 0) st16(ws + (2u << 20) + b * 16, v4u{tstart, tag, tstart, tag});
            {
                v4u v2[2]; const char* base = ws + OFF_XQ + g * 2048 + l * 16; const char* const pp[2] = {base, base + 1024};
                gather<2>(v2, pp, tag);
                *(v2u*)(qs + 2 * l) = v2u{v2[0].x, v2[0].z};
                *(v2u*)(qs + 128 + 2 * l) = v2u{v2[1].x, v2[1].z};
            }
            TS(2);
            bar();
            bar();
            {
                const v4f* q = (const v4f*)(st + (l & 3) * 16);
                const v4f s0 = q[0], s1 = q[1], s2 = q[2], s3 = q[3];
                const float mc = fmaxf(fmaxf(fmaxf(s0[0], s0[2]), fmaxf(s1[0], s1[2])), fmaxf(fmaxf(s2[0], s2[2]), fmaxf(s3[0], s3[2])));
                float lc = 0.f;
                lc += s0[1] * __builtin_amdgcn_exp2f((s0[0] - mc) * 1.4426950408889634f); lc += s0[3] * __builtin_amdgcn_exp2f((s0[2] - mc) * 1.4426950408889634f);
                lc += s1[1] * __builtin_amdgcn_exp2f((s1[0] - mc) * 1.4426950408889634f); lc += s1[3] * __builtin_amdgcn_exp2f((s1[2] - mc) * 1.4426950408889634f);
                lc += s2[1] * __builtin_amdgcn_exp2f((s2[0] - mc) * 1.4426950408889634f); lc += s2[3] * __builtin_amdgcn_exp2f((s2[2] - mc) * 1.4426950408889634f);
                lc += s3[1] * __builtin_amdgcn_exp2f((s3[0] - mc) * 1.4426950408889634f); lc += s3[3] * __builtin_amdgcn_exp2f((s3[2] - mc) * 1.4426950408889634f);
                const float z = mc * 1.4426950408889634f + __builtin_amdgcn_logf(lc);
                const float z1 = dpp<QP1>(z);
                if (l == 0 || l == 2) st16(ws + OFF_XS + (g * 16 + s) * 128 + l * 8, v4u{asu(z), tag, asu(z1), tag});
            }
            TS(4);
            bar();
            if (isR) { bar(); pub_bf16(ws + OFF_XM + (xcd * 8 + idx - 7) * 128, red32(red0, l), tag, l); TS(7); }
            else if (isM) { bar(); bar(); bar(); }
        }
    }
    TSDUMP();
}

extern "C" void kernel_launch(void* const* d_in, const int* in_sizes, int n_in, void* d_out, int out_size, void* d_ws, size_t ws_size, hipStream_t stream) {
    fused_kernel<<<256, 576, 0, stream>>>((const u16*)d_in[0], (const u16*)d_in[1], (const u16*)d_in[2], (const u16*)d_in[3], (const u16*)d_in[4],
        (const u16*)d_in[6], (const u16*)d_in[7], (const u16*)d_in[8], (const u16*)d_in[9], (const u16*)d_in[10], (u16*)d_out, (char*)d_ws);
}
```

```cpp
#include <hip/hip_runtime.h>
#include <stdint.h>

typedef unsigned u32;
typedef unsigned short u16;
typedef u32 v4u __attribute__((ext_vector_type(4)));
typedef u32 v2u __attribute__((ext_vector_type(2)));
typedef float v4f __attribute__((ext_vector_type(4)));
typedef float v2f __attribute__((ext_vector_type(2)));
typedef short v4s __attribute__((ext_vector_type(4)));
typedef __bf16 v8bf __attribute__((ext_vector_type(8)));
typedef __bf16 v2bf __attribute__((ext_vector_type(2)));
#define GAS __attribute__((address_space(1)))

#define DLY_A 0
#define DLY_WO 0

constexpr u32 OFF_EP = 0;
constexpr u32 OFF_XH = 4096;
constexpr u32 OFF_XM = 8192;
constexpr u32 OFF_XQ = 16384;
constexpr u32 OFF_XS = 32768;
constexpr u32 OFF_XW = 49152;
constexpr u32 OFF_XA = 131072;
constexpr u32 OFF_XD = 393216;
constexpr u32 OFF_DBG = 1u << 20;
constexpr int SPIN_MAX = 1 << 16;

__device__ __forceinline__ float asf(u32 v) { return __uint_as_float(v); }
__device__ __forceinline__ u32 asu(float v) { return __float_as_uint(v); }
__device__ __forceinline__ float lo16(u32 v) { return asf(v << 16); }
__device__ __forceinline__ float hi16(u32 v) { return asf(v & 0xffff0000u); }
__device__ __forceinline__ u32 pk2(float a, float b) { v2f t = {a, b}; v2bf r = __builtin_convertvector(t, v2bf); return __builtin_bit_cast(u32, r); }
__device__ __forceinline__ float rbf(float a) { return lo16(pk2(a, a) & 0xffffu); }
__device__ __forceinline__ v4u ldnt(const void* p) { return __builtin_nontemporal_load((const GAS v4u*)p); }
__device__ __forceinline__ void st16(void* p, v4u v) { asm volatile("global_store_dwordx4 %0, %1, off sc1\n s_nop 1" : : "v"(p), "v"(v) : "memory"); }
__device__ __forceinline__ v4u ld16(const void* p) { v4u v; asm volatile("global_load_dwordx4 %0, %1, off sc1" : "=v"(v) : "v"(p) : "memory"); return v; }
__device__ __forceinline__ void bar() { asm volatile("s_waitcnt lgkmcnt(0)\n s_barrier" ::: "memory"); }
template <int C> __device__ __forceinline__ float dpp(float x) { return asf(__builtin_amdgcn_update_dpp(0, asu(x), C, 0xf, 0xf, true)); }
#define QP1 0xB1
#define QP2 0x4E
#define ROR(n) (0x120 + (n))
__device__ __forceinline__ float x16add(float x) { auto r = __builtin_amdgcn_permlane16_swap(asu(x), asu(x), false, false); return asf(r[0]) + asf(r[1]); }
__device__ __forceinline__ float x32add(float x) { auto r = __builtin_amdgcn_permlane32_swap(asu(x), asu(x), false, false); return asf(r[0]) + asf(r[1]); }
__device__ __forceinline__ float x16max(float x) { auto r = __builtin_amdgcn_permlane16_swap(asu(x), asu(x), false, false); return fmaxf(asf(r[0]), asf(r[1])); }
__device__ __forceinline__ float x32max(float x) { auto r = __builtin_amdgcn_permlane32_swap(asu(x), asu(x), false, false); return fmaxf(asf(r[0]), asf(r[1])); }

template <int N> __device__ __forceinline__ void gather(v4u (&v)[N], const char* const (&p)[N], u32 tag) {
    for (int spin = 0; spin < SPIN_MAX; ++spin) {
#pragma unroll
        for (int i = 0; i < N; ++i) v[i] = ld16(p[i]);
        asm volatile("s_waitcnt vmcnt(0)" ::: "memory");
        u32 bad = 0;
#pragma unroll
        for (int i = 0; i < N; ++i) { asm volatile("" : "+v"(v[i])); bad |= (v[i].y ^ tag) | (v[i].w ^ tag); }
        if (__all(bad == 0)) break;
    }
}
__device__ __forceinline__ void perm4(v2u (&op)[8], const v4u (&r)[4]) {
#pragma unroll
    for (int j = 0; j < 8; ++j) {
        const u32 sel = (j & 1) ? 0x07060302u : 0x05040100u;
        op[j].x = __builtin_amdgcn_perm(r[1][j >> 1], r[0][j >> 1], sel);
        op[j].y = __builtin_amdgcn_perm(r[3][j >> 1], r[2][j >> 1], sel);
    }
}
__device__ __forceinline__ v4f mfma16(v2u a, v2u b) {
    return __builtin_amdgcn_mfma_f32_16x16x16bf16_1k(__builtin_bit_cast(v4s, a), __builtin_bit_cast(v4s, b), v4f{0.f, 0.f, 0.f, 0.f}, 0, 0, 0);
}
__device__ __forceinline__ v4f mfma32(v4u a, v4u b, v4f c) {
    return __builtin_amdgcn_mfma_f32_16x16x32_bf16(__builtin_bit_cast(v8bf, a), __builtin_bit_cast(v8bf, b), c, 0, 0, 0);
}
__device__ __forceinline__ void gemv32(float* red, v2u a, const v2u (&op)[8], int w, int l) {
    v4f d[8];
#pragma unroll
    for (int j = 0; j < 8; ++j) d[j] = mfma16(a, op[j]);
    if (((l >> 2) & 3) == (l >> 4)) {
        float* q = red + (8 * (l & 3)) * 36 + 4 * w + (l >> 4);
#pragma unroll
        for (int j = 0; j < 8; ++j) q[j * 36] = d[j][0];
    }
}
__device__ __forceinline__ float red32(const float* red, int l) {
    const v4f* q = (const v4f*)(red + (l & 31) * 36 + 16 * (l >> 5));
    v4f s = (q[0] + q[1]) + (q[2] + q[3]);
    return x32add((s[0] + s[1]) + (s[2] + s[3]));
}
__device__ __forceinline__ void pub_bf16(char* line, float r, u32 tag, int l) {
    float r1 = dpp<QP1>(r);
    u32 p = pk2(r, r1);
    u32 p2 = __builtin_amdgcn_update_dpp(0, p, QP2, 0xf, 0xf, true);
    if (l < 32 && (l & 3) == 0) st16(line + l * 4, v4u{p, tag, p2, tag});
}
__device__ __forceinline__ float silu_bf(float v) { return v * __builtin_amdgcn_rcpf(1.f + __expf(-v)); }

#define TS(i)
#define TSDUMP()

__global__ void __launch_bounds__(576) fused_kernel(const u16* __restrict__ xin, const u16* __restrict__ kc, const u16* __restrict__ vc,
        const u16* __restrict__ wq, const u16* __restrict__ wo, const u16* __restrict__ wg, const u16* __restrict__ wu,
        const u16* __restrict__ wd, const u16* __restrict__ cin, const u16* __restrict__ wmod, u16* __restrict__ out, char* __restrict__ ws) {
    __shared__ __attribute__((aligned(16))) float red0[32 * 36];
    __shared__ __attribute__((aligned(16))) float red1[32 * 36];
    __shared__ __attribute__((aligned(16))) float red2[32 * 36];
    __shared__ __attribute__((aligned(16))) v4f po[8 * 128];
    __shared__ __attribute__((aligned(16))) u32 qs[256];
    __shared__ __attribute__((aligned(16))) float st[64];
    __shared__ __attribute__((aligned(16))) float wt[64];
    __shared__ __attribute__((aligned(16))) float lns[32];
    __shared__ __attribute__((aligned(16))) u32 hhs[16];
    const int b = blockIdx.x, tid = threadIdx.x, l = tid & 63;
    const int w = __builtin_amdgcn_readfirstlane(tid >> 6);
    const int xcd = b & 7, idx = b >> 3;
    TS(0);
    const u32 epoch = __hip_atomic_load((const GAS u32*)(ws + OFF_EP), __ATOMIC_RELAXED, __HIP_MEMORY_SCOPE_AGENT);
    const int r4 = 64 * w + (l & 60);
    const u32 wofs = (u32)(l & 3) * 16;

    if (idx >= 16) {
        const int c = idx - 16, g = (xcd + 1) & 7;
        if (w < 8) {
            v4u rm[4], rq[4]; v2u cv;
            const bool ismod = c < 4;
            if (ismod) {
                cv = *(const GAS v2u*)(cin + r4);
                const char* p = (const char*)wmod + (size_t)r4 * 6144 + (xcd * 4 + c) * 64 + wofs;
#pragma unroll
                for (int i = 0; i < 4; ++i) rm[i] = ldnt(p + i * 6144);
            }
            const v4u xa = *(const GAS v4u*)(xin + 8 * l);
            const v2u xo = *(const GAS v2u*)(xin + r4);
            {
                const char* p = (const char*)wq + (size_t)r4 * 8192 + (g * 512 + c * 32) * 2 + wofs;
#pragma unroll
                for (int i = 0; i < 4; ++i) rq[i] = ldnt(p + i * 8192);
            }
            __builtin_amdgcn_sched_barrier(0);
            v2u op[8];
            if (ismod) {
                v2u a = {pk2(silu_bf(lo16(cv.x)), silu_bf(hi16(cv.x))), pk2(silu_bf(lo16(cv.y)), silu_bf(hi16(cv.y)))};
                perm4(op, rm);
                gemv32(red0, a, op, w, l);
                TS(1);
                bar();
            }
            float xv[8] = {lo16(xa.x), hi16(xa.x), lo16(xa.y), hi16(xa.y), lo16(xa.z), hi16(xa.z), lo16(xa.w), hi16(xa.w)};
            float s = ((xv[0] + xv[1]) + (xv[2] + xv[3])) + ((xv[4] + xv[5]) + (xv[6] + xv[7]));
            s += dpp<QP1>(s); s += dpp<QP2>(s); s += dpp<ROR(4)>(s); s += dpp<ROR(8)>(s); s = x32add(x16add(s));
            const float mean = s * (1.f / 512.f);
            float v = 0.f;
#pragma unroll
            for (int i = 0; i < 8; ++i) { float d = xv[i] - mean; v += d * d; }
            v += dpp<QP1>(v); v += dpp<QP2>(v); v += dpp<ROR(4)>(v); v += dpp<ROR(8)>(v); v = x32add(x16add(v));
            const float mb = rbf(mean);
            const float dn = rbf(__builtin_sqrtf(rbf(rbf(v * (1.f / 512.f)) + 1e-5f)));
            float n[4] = {rbf(rbf(lo16(xo.x) - mb) / dn), rbf(rbf(hi16(xo.x) - mb) / dn), rbf(rbf(lo16(xo.y) - mb) / dn), rbf(rbf(hi16(xo.y) - mb) / dn)};
            perm4(op, rq);
            const u32 tag = epoch * 1664525u + 1013904223u;
            TS(2);
            {
                v4u v2[2]; const char* const pp[2] = {ws + OFF_XH + r4 * 4, ws + OFF_XH + 2048 + r4 * 4};
                gather<2>(v2, pp, tag);
                TS(3);
                v2u a;
                a.x = pk2(rbf(n[0] * rbf(1.f + lo16(v2[1].x))) + lo16(v2[0].x), rbf(n[1] * rbf(1.f + hi16(v2[1].x))) + hi16(v2[0].x));
                a.y = pk2(rbf(n[2] * rbf(1.f + lo16(v2[1].z))) + lo16(v2[0].z), rbf(n[3] * rbf(1.f + hi16(v2[1].z))) + hi16(v2[0].z));
                gemv32(red1, a, op, w, l);
            }
            bar();
            TS(4);
            if (DLY_WO) __builtin_amdgcn_s_sleep(DLY_WO);
            {
                const char* p = (const char*)wo + (size_t)(g * 512 + 8 * (l >> 2) + w) * 1024 + c * 64 + wofs;
#pragma unroll
                for (int i = 0; i < 4; ++i) rq[i] = ldnt(p + i * 131072);
            }
            __builtin_amdgcn_sched_barrier(0);
            bar();
            TS(5);
            v4f wv[4];
#pragma unroll
            for (int i = 0; i < 4; ++i) wv[i] = *(const v4f*)(wt + (4 * (l & 3) + i) * 4);
            perm4(op, rq);
            TS(6);
            {
                v4u va[4];
                const char* base = ws + OFF_XA + (g * 16 + 4 * (l & 3)) * 2048 + (16 * w + (l >> 2)) * 16;
                const char* const pp[4] = {base, base + 2048, base + 4096, base + 6144};
                gather<4>(va, pp, tag);
                TS(7);
                v2f a01 = {0.f, 0.f}, a23 = {0.f, 0.f};
#pragma unroll
                for (int i = 0; i < 4; ++i) {
                    a01 += v2f{asf(va[i].x), lo16(va[i].x)} * v2f{wv[i][0], wv[i][1]};
                    a23 += v2f{asf(va[i].z), lo16(va[i].z)} * v2f{wv[i][2], wv[i][3]};
                }
                float o1 = a01[0], o0 = a01[1], o3 = a23[0], o2 = a23[1];
                o0 += dpp<QP1>(o0); o1 += dpp<QP1>(o1); o2 += dpp<QP1>(o2); o3 += dpp<QP1>(o3);
                o0 += dpp<QP2>(o0); o1 += dpp<QP2>(o1); o2 += dpp<QP2>(o2); o3 += dpp<QP2>(o3);
                gemv32(red2, v2u{pk2(o0, o1), pk2(o2, o3)}, op, w, l);
            }
            bar();
            TS(8);
        } else {
            const u32 tag = epoch * 1664525u + 1013904223u;
            if (c < 4) { bar(); pub_bf16(ws + OFF_XH + (xcd * 4 + c) * 128, red32(red0, l), tag, l); TS(1); }
            bar();
            pub_bf16(ws + OFF_XQ + (g * 16 + c) * 128, red32(red1, l), tag, l);
            TS(4);
            {
                v4u vs[1]; const char* const pp[1] = {ws + OFF_XS + (g * 16 + (l & 15)) * 128 + ((l >> 4) & 1) * 16};
                gather<1>(vs, pp, tag);
                TS(5);
                float z0 = asf(vs[0].x), z1 = asf(vs[0].z), m0 = z0, m1 = z1;
                m0 = fmaxf(m0, dpp<QP1>(m0)); m1 = fmaxf(m1, dpp<QP1>(m1)); m0 = fmaxf(m0, dpp<QP2>(m0)); m1 = fmaxf(m1, dpp<QP2>(m1));
                m0 = fmaxf(m0, dpp<ROR(4)>(m0)); m1 = fmaxf(m1, dpp<ROR(4)>(m1)); m0 = fmaxf(m0, dpp<ROR(8)>(m0)); m1 = fmaxf(m1, dpp<ROR(8)>(m1));
                float e0 = __builtin_amdgcn_exp2f(z0 - m0), e1 = __builtin_amdgcn_exp2f(z1 - m1), s0 = e0, s1 = e1;
                s0 += dpp<QP1>(s0); s1 += dpp<QP1>(s1); s0 += dpp<QP2>(s0); s1 += dpp<QP2>(s1);
                s0 += dpp<ROR(4)>(s0); s1 += dpp<ROR(4)>(s1); s0 += dpp<ROR(8)>(s0); s1 += dpp<ROR(8)>(s1);
                if (l < 32) *(v2f*)(wt + (l & 15) * 4 + (l >> 4) * 2) = v2f{e1 / s1, e0 / s0};
            }
            bar();
            bar();
            {
                float r = red32(red2, l), r1 = dpp<QP1>(r);
                if (l < 32 && (l & 1) == 0) st16(ws + OFF_XW + (c * 8 + g) * 256 + l * 8, v4u{asu(r), tag, asu(r1), tag});
            }
            TS(8);
            if (c < 4) {
                const int col = (xcd * 4 + c) * 16 + 2 * (l & 7), pg = l >> 3;
                const u32 xr = *(const GAS u32*)(xin + col);
                v4u v3[3];
                const char* const pp[3] = {ws + OFF_XW + ((col >> 5) * 8 + pg) * 256 + (col & 31) * 8, ws + OFF_XM + (col & ~3) * 4, ws + OFF_XM + 6144 + (col & ~3) * 4};
                gather<3>(v3, pp, tag);
                float a0 = asf(v3[0].x), a1 = asf(v3[0].z);
                a0 += dpp<ROR(8)>(a0); a1 += dpp<ROR(8)>(a1); a0 = x32add(x16add(a0)); a1 = x32add(x16add(a1));
                const u32 ga = (l & 1) ? v3[1].z : v3[1].x, gb = (l & 1) ? v3[2].z : v3[2].x;
                const float x20 = rbf(lo16(xr) + rbf(lo16(ga) * rbf(a0))), x21 = rbf(hi16(xr) + rbf(hi16(ga) * rbf(a1)));
                TS(9);
                v4u vd[7];
                const char* base = ws + OFF_XD + pg * 4096 + col * 8;
                const char* const pd[7] = {base, base + 32768, base + 65536, base + 98304, base + 131072, base + 163840, base + 196608};
                gather<7>(vd, pd, tag);
                TS(10);
                float y0 = 0.f, y1 = 0.f;
#pragma unroll
                for (int i = 0; i < 7; ++i) { y0 += asf(vd[i].x); y1 += asf(vd[i].z); }
                y0 += dpp<ROR(8)>(y0); y1 += dpp<ROR(8)>(y1); y0 = x32add(x16add(y0)); y1 = x32add(x16add(y1));
                const u32 o = pk2(x20 + rbf(lo16(gb) * rbf(y0)), x21 + rbf(hi16(gb) * rbf(y1)));
                if (l < 8) *(GAS u32*)(out + col) = o;
                if (b == 128 && l == 0) __hip_atomic_store((GAS u32*)(ws + OFF_EP), tag, __ATOMIC_RELAXED, __HIP_MEMORY_SCOPE_AGENT);
                TS(11);
            }
        }
    } else {
        const int g = xcd, s = idx;
        const bool isM = idx < 7, isR = idx >= 7 && idx < 15;
        if (w < 8) {
            if (DLY_A) __builtin_amdgcn_s_sleep(DLY_A);
            v4u rk[8], rv[8];
            const size_t kb = ((size_t)g * 65536 + 61440 + 256 * s + 32 * w) * 256;
            {
                const char* p = (const char*)kc + kb + (l & 15) * 256 + (l >> 4) * 16;
#pragma unroll
                for (int i = 0; i < 8; ++i) rk[i] = ldnt(p + (i >> 2) * 4096 + (i & 3) * 64);
                const char* q = (const char*)vc + kb + (l >> 4) * 1024 + (l & 15) * 16;
#pragma unroll
                for (int i = 0; i < 8; ++i) rv[i] = ldnt(q + (i >> 2) * 4096 + (i & 3) * 256);
            }
            __builtin_amdgcn_sched_barrier(0);
            TS(1);
            bar();
            TS(2);
            v4f sa = {0.f, 0.f, 0.f, 0.f}, sb = sa;
#pragma unroll
            for (int t = 0; t < 4; ++t) {
                const v4u qv = *(const v4u*)(qs + (l & 3) * 64 + (4 * t + (l >> 4)) * 4);
                sa = mfma32(rk[t], qv, sa); sb = mfma32(rk[4 + t], qv, sb);
            }
            float e[8], mw;
#pragma unroll
            for (int i = 0; i < 4; ++i) { e[i] = rbf(rbf(sa[i]) * 0.08838834764831845f); e[4 + i] = rbf(rbf(sb[i]) * 0.08838834764831845f); }
            mw = fmaxf(fmaxf(fmaxf(e[0], e[1]), fmaxf(e[2], e[3])), fmaxf(fmaxf(e[4], e[5]), fmaxf(e[6], e[7])));
            mw = x32max(x16max(mw));
            float lw = 0.f;
#pragma unroll
            for (int i = 0; i < 8; ++i) { e[i] = __builtin_amdgcn_exp2f((e[i] - mw) * 1.4426950408889634f); lw += e[i]; }
            lw = x32add(x16add(lw));
            if (l < 4) *(v2f*)(st + l * 16 + w * 2) = v2f{mw, lw};
            TS(3);
            bar();
            v4u pa; float linv;
            {
                const v4f* q = (const v4f*)(st + (l & 3) * 16);
                const v4f s0 = q[0], s1 = q[1], s2 = q[2], s3 = q[3];
                const float mc = fmaxf(fmaxf(fmaxf(s0[0], s0[2]), fmaxf(s1[0], s1[2])), fmaxf(fmaxf(s2[0], s2[2]), fmaxf(s3[0], s3[2])));
                const float f = __builtin_amdgcn_exp2f((mw - mc) * 1.4426950408889634f);
                pa = v4u{pk2(e[0] * f, e[1] * f), pk2(e[2] * f, e[3] * f), pk2(e[4] * f, e[5] * f), pk2(e[6] * f, e[7] * f)};
                float lc = 0.f;
                lc += s0[1] * __builtin_amdgcn_exp2f((s0[0] - mc) * 1.4426950408889634f); lc += s0[3] * __builtin_amdgcn_exp2f((s0[2] - mc) * 1.4426950408889634f);
                lc += s1[1] * __builtin_amdgcn_exp2f((s1[0] - mc) * 1.4426950408889634f); lc += s1[3] * __builtin_amdgcn_exp2f((s1[2] - mc) * 1.4426950408889634f);
                lc += s2[1] * __builtin_amdgcn_exp2f((s2[0] - mc) * 1.4426950408889634f); lc += s2[3] * __builtin_amdgcn_exp2f((s2[2] - mc) * 1.4426950408889634f);
                lc += s3[1] * __builtin_amdgcn_exp2f((s3[0] - mc) * 1.4426950408889634f); lc += s3[3] * __builtin_amdgcn_exp2f((s3[2] - mc) * 1.4426950408889634f);
                linv = 1.f / lc;
            }
            const float li0 = dpp<0x00>(linv), li1 = dpp<0x55>(linv), li2 = dpp<0xAA>(linv), li3 = dpp<0xFF>(linv);
            TS(4);
            v4u vo[8];
#pragma unroll
            for (int pr = 0; pr < 4; ++pr)
#pragma unroll
                for (int j = 0; j < 8; ++j) vo[j][pr] = __builtin_amdgcn_perm(rv[2 * pr + 1][j >> 1], rv[2 * pr][j >> 1], (j & 1) ? 0x07060302u : 0x05040100u);
            TS(5);
            v4u r0[4], r1[4], r2[4]; v2u cv;
            const int jm = xcd * 7 + idx;
            if (isM) {
                const size_t o = (size_t)r4 * 3584 + jm * 64 + wofs;
#pragma unroll
                for (int i = 0; i < 4; ++i) r0[i] = ldnt((const char*)wg + o + i * 3584);
#pragma unroll
                for (int i = 0; i < 4; ++i) r1[i] = ldnt((const char*)wu + o + i * 3584);
                const char* p = (const char*)wd + (size_t)(32 * jm + 4 * (l >> 3)) * 1024 + 128 * w + (l & 7) * 16;
#pragma unroll
                for (int i = 0; i < 4; ++i) r2[i] = ldnt(p + i * 1024);
                cv = *(const GAS v2u*)(xin + r4);
            } else if (isR) {
                const char* p = (const char*)wmod + (size_t)r4 * 6144 + 2048 + (xcd * 8 + idx - 7) * 64 + wofs;
#pragma unroll
                for (int i = 0; i < 4; ++i) r0[i] = ldnt(p + i * 6144);
                cv = *(const GAS v2u*)(cin + r4);
            }
            __builtin_amdgcn_sched_barrier(0);
            {
                v4f d[8];
#pragma unroll
                for (int j = 0; j < 8; ++j) d[j] = mfma32(pa, vo[j], v4f{0.f, 0.f, 0.f, 0.f});
                if (l < 16) {
#pragma unroll
                    for (int j = 0; j < 8; ++j) po[(w * 8 + j) * 16 + l] = d[j];
                }
            }
            bar();
            const u32 tag = epoch * 1664525u + 1013904223u;
            if (w < 2) {
                v4f o = po[tid];
#pragma unroll
                for (int i = 1; i < 8; ++i) o += po[i * 128 + tid];
                const u32 l0 = pk2(o[0] * li0, o[2] * li2);
                const u32 b1 = asu(o[1] * li1), b3 = asu(o[3] * li3);
                const u32 q0 = l0 & 0xffffu, q2 = l0 >> 16;
                const u32 m1 = (b1 & 0x7fffffffu) + 0x8000u, m3 = (b3 & 0x7fffffffu) + 0x8000u;
                const u32 h1 = (m1 > q0 ? m1 - q0 : 0u) & 0x7fff0000u, h3 = (m3 > q2 ? m3 - q2 : 0u) & 0x7fff0000u;
                st16(ws + OFF_XA + (g * 16 + s) * 2048 + tid * 16, v4u{(b1 & 0x80000000u) | h1 | q0, tag, (b3 & 0x80000000u) | h3 | q2, tag});
            }
            TS(6);
            if (isR) {
                v2u op[8];
                v2u a = {pk2(silu_bf(lo16(cv.x)), silu_bf(hi16(cv.x))), pk2(silu_bf(lo16(cv.y)), silu_bf(hi16(cv.y)))};
                perm4(op, r0);
                gemv32(red0, a, op, w, l);
                bar();
                TS(7);
            } else if (isM) {
                v2u og[8], ou[8], od[8];
                perm4(og, r0); perm4(ou, r1); perm4(od, r2);
                TS(7);
                v4u vm[3];
                { const char* base = ws + OFF_XM + r4 * 4; const char* const pp[3] = {base, base + 2048, base + 4096}; gather<3>(vm, pp, tag); }
                TS(8);
                float x2[4];
                {
                    v4u vw[4];
                    const char* base = ws + OFF_XW + ((r4 >> 5) * 8 + 2 * (l & 3)) * 256 + (r4 & 31) * 8;
                    const char* const pp[4] = {base, base + 16, base + 256, base + 272};
                    gather<4>(vw, pp, tag);
                    TS(9);
                    float a0 = asf(vw[0].x) + asf(vw[2].x), a1 = asf(vw[0].z) + asf(vw[2].z), a2 = asf(vw[1].x) + asf(vw[3].x), a3 = asf(vw[1].z) + asf(vw[3].z);
                    a0 += dpp<QP1>(a0); a1 += dpp<QP1>(a1); a2 += dpp<QP1>(a2); a3 += dpp<QP1>(a3);
                    a0 += dpp<QP2>(a0); a1 += dpp<QP2>(a1); a2 += dpp<QP2>(a2); a3 += dpp<QP2>(a3);
                    x2[0] = rbf(lo16(cv.x) + rbf(lo16(vm[0].x) * rbf(a0))); x2[1] = rbf(hi16(cv.x) + rbf(hi16(vm[0].x) * rbf(a1)));
                    x2[2] = rbf(lo16(cv.y) + rbf(lo16(vm[0].z) * rbf(a2))); x2[3] = rbf(hi16(cv.y) + rbf(hi16(vm[0].z) * rbf(a3)));
                }
                {
                    const float pv = asf(__builtin_amdgcn_readfirstlane(asu(x2[0])));
                    const float d0 = x2[0] - pv, d1 = x2[1] - pv, d2 = x2[2] - pv, d3 = x2[3] - pv;
                    float s1 = (d0 + d1) + (d2 + d3), s2 = (d0 * d0 + d1 * d1) + (d2 * d2 + d3 * d3);
                    s1 += dpp<ROR(4)>(s1); s2 += dpp<ROR(4)>(s2); s1 += dpp<ROR(8)>(s1); s2 += dpp<ROR(8)>(s2);
                    s1 = x32add(x16add(s1)); s2 = x32add(x16add(s2));
                    if (l == 0) *(v4f*)(lns + w * 4) = v4f{pv, s1, s2, 0.f};
                }
                bar();
                {
                    const v4f t = *(const v4f*)(lns + (l & 7) * 4);
                    float a = 64.f * t[0] + t[1];
                    a += dpp<QP1>(a); a += dpp<QP2>(a); a += dpp<ROR(4)>(a) ;
                    const float mean = a * (1.f / 512.f), ee = mean - t[0];
                    float v = t[2] - 2.f * ee * t[1] + 64.f * ee * ee;
                    v += dpp<QP1>(v); v += dpp<QP2>(v); v += dpp<ROR(4)>(v);
                    const float mb = rbf(mean);
                    const float dn = rbf(__builtin_sqrtf(rbf(rbf(fmaxf(v, 0.f) * (1.f / 512.f)) + 1e-5f)));
                    const float ri = 1.f / dn;
                    v2u a2;
                    a2.x = pk2(rbf(rbf(rbf(x2[0] - mb) * ri) * rbf(1.f + lo16(vm[2].x))) + lo16(vm[1].x), rbf(rbf(rbf(x2[1] - mb) * ri) * rbf(1.f + hi16(vm[2].x))) + hi16(vm[1].x));
                    a2.y = pk2(rbf(rbf(rbf(x2[2] - mb) * ri) * rbf(1.f + lo16(vm[2].z))) + lo16(vm[1].z), rbf(rbf(rbf(x2[3] - mb) * ri) * rbf(1.f + hi16(vm[2].z))) + hi16(vm[1].z));
                    gemv32(red0, a2, og, w, l);
                    gemv32(red1, a2, ou, w, l);
                }
                bar();
                TS(10);
                {
                    const float* rr = (l < 32) ? red0 : red1;
                    const v4f t = *(const v4f*)(rr + (4 * w + ((l >> 3) & 3)) * 36 + (l & 7) * 4);
                    float a = (t[0] + t[1]) + (t[2] + t[3]);
                    a += dpp<QP1>(a); a += dpp<QP2>(a); a += dpp<0x141>(a);
                    auto r = __builtin_amdgcn_permlane32_swap(asu(a), asu(a), false, false);
                    const float gg = rbf(asf(r[0])), uu = rbf(asf(r[1]));
                    const float tt = 0.7978845608028654f * (gg + 0.044715f * gg * gg * gg);
                    const float th = 1.f - 2.f * __builtin_amdgcn_rcpf(1.f + __builtin_amdgcn_exp2f(tt * 2.885390081777927f));
                    const float hv = rbf(0.5f * gg * (1.f + th)) * uu;
                    const u32 hp = pk2(hv, dpp<ROR(8)>(hv));
                    if ((l & 47) == 0) hhs[2 * w + (l >> 4)] = hp;
                }
                bar();
                {
                    const v2u a = *(const v2u*)(hhs + 2 * (2 * (l >> 4) + (l & 1)));
                    float y[8];
#pragma unroll
                    for (int j = 0; j < 8; ++j) { const v4f d = mfma16(a, od[j]); y[j] = d[0] + dpp<ROR(8)>(d[1]); }
                    const int q = l >> 4;
                    const float y0 = q == 0 ? y[0] : q == 1 ? y[2] : q == 2 ? y[4] : y[6];
                    const float y1 = q == 0 ? y[1] : q == 1 ? y[3] : q == 2 ? y[5] : y[7];
                    if ((l & 8) == 0) st16(ws + OFF_XD + jm * 4096 + (64 * w + 8 * (l & 7) + 2 * q) * 8, v4u{asu(y0), tag, asu(y1), tag});
                }
                TS(11);
            }
        } else {
            if (idx == 15) asm volatile("buffer_wbl2 sc1\n s_waitcnt vmcnt(0)" ::: "memory");
            const u32 tag = epoch * 1664525u + 1013904223u;
            {
                v4u v2[2]; const char* base = ws + OFF_XQ + g * 2048 + l * 16; const char* const pp[2] = {base, base + 1024};
                gather<2>(v2, pp, tag);
                *(v2u*)(qs + 2 * l) = v2u{v2[0].x, v2[0].z};
                *(v2u*)(qs + 128 + 2 * l) = v2u{v2[1].x, v2[1].z};
            }
            TS(2);
            bar();
            bar();
            {
                const v4f* q = (const v4f*)(st + (l & 3) * 16);
                const v4f s0 = q[0], s1 = q[1], s2 = q[2], s3 = q[3];
                const float mc = fmaxf(fmaxf(fmaxf(s0[0], s0[2]), fmaxf(s1[0], s1[2])), fmaxf(fmaxf(s2[0], s2[2]), fmaxf(s3[0], s3[2])));
                float lc = 0.f;
                lc += s0[1] * __builtin_amdgcn_exp2f((s0[0] - mc) * 1.4426950408889634f); lc += s0[3] * __builtin_amdgcn_exp2f((s0[2] - mc) * 1.4426950408889634f);
                lc += s1[1] * __builtin_amdgcn_exp2f((s1[0] - mc) * 1.4426950408889634f); lc += s1[3] * __builtin_amdgcn_exp2f((s1[2] - mc) * 1.4426950408889634f);
                lc += s2[1] * __builtin_amdgcn_exp2f((s2[0] - mc) * 1.4426950408889634f); lc += s2[3] * __builtin_amdgcn_exp2f((s2[2] - mc) * 1.4426950408889634f);
                lc += s3[1] * __builtin_amdgcn_exp2f((s3[0] - mc) * 1.4426950408889634f); lc += s3[3] * __builtin_amdgcn_exp2f((s3[2] - mc) * 1.4426950408889634f);
                const float z = mc * 1.4426950408889634f + __builtin_amdgcn_logf(lc);
                const float z1 = dpp<QP1>(z);
                if (l == 0 || l == 2) st16(ws + OFF_XS + (g * 16 + s) * 128 + l * 8, v4u{asu(z), tag, asu(z1), tag});
            }
            TS(4);
            bar();
            if (isR) { bar(); pub_bf16(ws + OFF_XM + (xcd * 8 + idx - 7) * 128, red32(red0, l), tag, l); TS(7); }
            else if (isM) { bar(); bar(); bar(); }
        }
    }
    TSDUMP();
}

extern "C" void kernel_launch(void* const* d_in, const int* in_sizes, int n_in, void* d_out, int out_size, void* d_ws, size_t ws_size, hipStream_t stream) {
    fused_kernel<<<256, 576, 0, stream>>>((const u16*)d_in[0], (const u16*)d_in[1], (const u16*)d_in[2], (const u16*)d_in[3], (const u16*)d_in[4],
        (const u16*)d_in[6], (const u16*)d_in[7], (const u16*)d_in[8], (const u16*)d_in[9], (const u16*)d_in[10], (u16*)d_out, (char*)d_ws);
}
```

```cpp
#include <hip/hip_runtime.h>
#include <stdint.h>

typedef unsigned u32;
typedef unsigned short u16;
typedef u32 v4u __attribute__((ext_vector_type(4)));
typedef u32 v2u __attribute__((ext_vector_type(2)));
typedef float v4f __attribute__((ext_vector_type(4)));
typedef float v2f __attribute__((ext_vector_type(2)));
typedef __bf16 v8bf __attribute__((ext_vector_type(8)));
typedef __bf16 v2bf __attribute__((ext_vector_type(2)));
#define GAS __attribute__((address_space(1)))
#define LAS __attribute__((address_space(3)))

#define T_M 260
#define T_R 150
#define T_R7 150
#define T_QP 150
#define T_XW 0
#define GROT 1

constexpr u32 OFF_EP = 0;
constexpr u32 OFF_XQ = 16384;
constexpr u32 OFF_XS = 32768;
constexpr u32 OFF_XM = 65536;
constexpr u32 OFF_XA = 327680;
constexpr u32 OFF_XW = 589824;
constexpr u32 OFF_XD = 851968;
constexpr u32 OFF_DBG = 2u << 20;
constexpr int SPIN_MAX = 1 << 16;
constexpr u32 L_Q = 65536, L_ST = 67072, L_F = 67200, L_SIZE = 86016;
constexpr u32 L_R0 = 0, L_R1 = 4096, L_LN = 8192, L_RG = 8448;

__device__ __forceinline__ float asf(u32 v) { return __uint_as_float(v); }
__device__ __forceinline__ u32 asu(float v) { return __float_as_uint(v); }
__device__ __forceinline__ float lo16(u32 v) { return asf(v << 16); }
__device__ __forceinline__ float hi16(u32 v) { return asf(v & 0xffff0000u); }
__device__ __forceinline__ u32 pk2(float a, float b) { v2f t = {a, b}; v2bf r = __builtin_convertvector(t, v2bf); return __builtin_bit_cast(u32, r); }
__device__ __forceinline__ float rbf(float a) { return lo16(pk2(a, a)); }
__device__ __forceinline__ v4u ldnt(const char* sb, u32 vo) { return __builtin_nontemporal_load((const GAS v4u*)(sb + vo)); }
template <int IMM> __device__ __forceinline__ void st16(char* sb, u32 vo, v4u v) { asm volatile("global_store_dwordx4 %0, %1, %2 offset:%3 sc1\n s_nop 1" : : "v"(vo), "v"(v), "s"(sb), "n"(IMM) : "memory"); }
__device__ __forceinline__ void st8(char* sb, u32 vo, v2u v) { asm volatile("global_store_dwordx2 %0, %1, %2 sc1\n s_nop 1" : : "v"(vo), "v"(v), "s"(sb) : "memory"); }
template <int IMM> __device__ __forceinline__ void ld16(v4u& v, const char* sb, u32 vo) { asm volatile("global_load_dwordx4 %0, %1, %2 offset:%3 sc1" : "=v"(v) : "v"(vo), "s"(sb), "n"(IMM) : "memory"); }
template <int N> __device__ __forceinline__ void wvm(v4u& v) { asm volatile("s_waitcnt vmcnt(%1)" : "+v"(v) : "n"(N) : "memory"); }
__device__ __forceinline__ void bar() { asm volatile("s_waitcnt lgkmcnt(0)\n s_barrier" ::: "memory"); }
template <int C> __device__ __forceinline__ u32 dppu(u32 x) { return __builtin_amdgcn_update_dpp(0, x, C, 0xf, 0xf, true); }
template <int C> __device__ __forceinline__ float dpp(float x) { return asf(dppu<C>(asu(x))); }
#define QP1 0xB1
#define QP2 0x4E
#define QID 0xE4
#define QB(n) ((n) * 0x55)
#define SHL(n) (0x100 + (n))
#define ROR(n) (0x120 + (n))
#define NBC(n) (0x150 + (n))
__device__ __forceinline__ float x16add(float x) { auto r = __builtin_amdgcn_permlane16_swap(asu(x), asu(x), false, false); return asf(r[0]) + asf(r[1]); }
__device__ __forceinline__ float x32add(float x) { auto r = __builtin_amdgcn_permlane32_swap(asu(x), asu(x), false, false); return asf(r[0]) + asf(r[1]); }
__device__ __forceinline__ float x16max(float x) { auto r = __builtin_amdgcn_permlane16_swap(asu(x), asu(x), false, false); return fmaxf(asf(r[0]), asf(r[1])); }
__device__ __forceinline__ float x32max(float x) { auto r = __builtin_amdgcn_permlane32_swap(asu(x), asu(x), false, false); return fmaxf(asf(r[0]), asf(r[1])); }
__device__ __forceinline__ u32 badtag(const v4u& v, u32 tag) { return (v.y ^ tag) | (v.w ^ tag); }
__device__ __forceinline__ v4f mfma32(v4u a, v4u b, v4f c) {
    return __builtin_amdgcn_mfma_f32_16x16x32_bf16(__builtin_bit_cast(v8bf, a), __builtin_bit_cast(v8bf, b), c, 0, 0, 0);
}
__device__ __forceinline__ void perm8(v4u (&op)[8], const v4u* r) {
#pragma unroll
    for (int p = 0; p < 4; ++p)
#pragma unroll
        for (int j = 0; j < 8; ++j) op[j][p] = __builtin_amdgcn_perm(r[2 * p + 1][j >> 1], r[2 * p][j >> 1], (j & 1) ? 0x07060302u : 0x05040100u);
}
__device__ __forceinline__ void pin8(v4u (&op)[8]) {
    asm volatile("" : "+v"(op[0]), "+v"(op[1]), "+v"(op[2]), "+v"(op[3]), "+v"(op[4]), "+v"(op[5]), "+v"(op[6]), "+v"(op[7]));
}
__device__ __forceinline__ float silu_f(float v) { return v * __builtin_amdgcn_rcpf(1.f + __expf(-v)); }
__device__ __forceinline__ u32 now() { return (u32)__builtin_amdgcn_s_memrealtime(); }
__device__ __forceinline__ void wait_until(u32 t0, u32 d) { if (d) while (now() - t0 < d) __builtin_amdgcn_s_sleep(2); }
template <int NS> __device__ __forceinline__ v2f redcols(LAS char* red, int l) {
    const volatile LAS v2f* q = (const volatile LAS v2f*)(red + (l >> 5) * (NS / 2) * 256 + (l & 31) * 8);
    v2f a[NS / 2];
#pragma unroll
    for (int i = 0; i < NS / 2; ++i) a[i] = q[i * 32];
#pragma unroll
    for (int s = NS / 4; s > 0; s >>= 1)
#pragma unroll
        for (int i = 0; i < s; ++i) a[i] += a[i + s];
    return v2f{x32add(a[0][0]), x32add(a[0][1])};
}
__device__ __forceinline__ void gemv64(LAS char* red, v4u a, const v4u (&o0)[8], const v4u (&o1)[8], int w, int l) {
    float y[8];
#pragma unroll
    for (int j = 0; j < 8; ++j) {
        const v4f d0 = mfma32(a, o0[j], v4f{0.f, 0.f, 0.f, 0.f}), d1 = mfma32(a, o1[j], v4f{0.f, 0.f, 0.f, 0.f});
        y[j] = asf(__builtin_amdgcn_update_dpp(asu(d0[0]), asu(d1[0]), QID, 0xc, 0xf, false));
    }
    if (((l >> 3) & 1) == ((l >> 4) & 1)) {
        LAS v4f* q = (LAS v4f*)(red + (4 * w + (l >> 4)) * 256 + (l & 7) * 32);
        q[0] = v4f{y[0], y[1], y[2], y[3]}; q[1] = v4f{y[4], y[5], y[6], y[7]};
    }
}
template <int NB> __device__ __forceinline__ void mod_issue(v4u (&r)[8 * NB], u32 (&cp)[NB], const u16* wmod, const u16* cin, int col0, int row0, int l) {
#pragma unroll
    for (int k = 0; k < NB; ++k) cp[k] = *(const GAS u32*)(cin + row0 + 64 * k + 8 * (l >> 3) + 2 * (l & 3));
    const char* sb = (const char*)wmod + (size_t)row0 * 6144 + col0 * 2;
    const u32 vo = (u32)(l >> 3) * 49152u + (l & 7) * 16;
#pragma unroll
    for (int i = 0; i < 8 * NB; ++i) r[i] = ldnt(sb + ((i >> 3) * 64 + (i & 7)) * 6144, vo);
}
template <int NB> __device__ __forceinline__ void mod_compute(LAS char* red, const v4u (&r)[8 * NB], const u32 (&cp)[NB], int w, int l) {
    const u32 hm = (l & 1) ? 0xffff0000u : 0x0000ffffu;
    const int e2 = (l & 7) >> 1;
    const u32 m0 = e2 == 0 ? hm : 0u, m1 = e2 == 1 ? hm : 0u, m2 = e2 == 2 ? hm : 0u, m3 = e2 == 3 ? hm : 0u;
    v4f acc = {0.f, 0.f, 0.f, 0.f};
#pragma unroll
    for (int k = 0; k < NB; ++k) {
        const float s0 = silu_f(lo16(cp[k])), s1 = silu_f(hi16(cp[k]));
        const u32 d0 = pk2(s0, s0), d1 = pk2(s1, s1);
#define MODT(t) { const u32 x = dppu<QB((t) >> 1)>(((t) & 1) ? d1 : d0); acc = mfma32(v4u{x & m0, x & m1, x & m2, x & m3}, r[8 * k + (t)], acc); }
        MODT(0) MODT(1) MODT(2) MODT(3) MODT(4) MODT(5) MODT(6) MODT(7)
#undef MODT
    }
    if (((l >> 3) & 1) == (l >> 5)) *(LAS v4f*)(red + (2 * w + (l >> 5)) * 256 + (l & 7) * 32 + ((l >> 4) & 1) * 16) = acc;
}
__device__ __forceinline__ void pub_f32(char* sb, v2f r, u32 tag, int l) {
    const v4u v = {asu(r[0]), tag, asu(r[1]), tag};
    const u32 vo = (u32)(l >> 5) * 32768u + (l & 31) * 16;
    st16<0>(sb, vo, v); st16<0>(sb + 65536, vo, v); st16<0>(sb + 131072, vo, v); st16<0>(sb + 196608, vo, v);
}

#define TS(i)
#define TSDUMP()

__global__ void __launch_bounds__(320) fused_kernel(const u16* __restrict__ xin, const u16* __restrict__ kc, const u16* __restrict__ vc,
        const u16* __restrict__ wq, const u16* __restrict__ wo, const u16* __restrict__ wg, const u16* __restrict__ wu,
        const u16* __restrict__ wd, const u16* __restrict__ cin, const u16* __restrict__ wmod, u16* __restrict__ out, char* __restrict__ ws) {
    __shared__ __attribute__((aligned(16))) char lds_[L_SIZE];
    LAS char* const lds = (LAS char*)lds_;
    const int b = blockIdx.x, tid = threadIdx.x, l = tid & 63;
    const int w = __builtin_amdgcn_readfirstlane(tid >> 6);
    const int xcd = b & 7, idx = b >> 3;
    const u32 t0 = now();
    const u32 epoch = __hip_atomic_load((const GAS u32*)(ws + OFF_EP), __ATOMIC_RELAXED, __HIP_MEMORY_SCOPE_AGENT);
#define NEXT_EPOCH (__builtin_amdgcn_readfirstlane(epoch) * 1664525u + 1013904223u)
#define TAG (NEXT_EPOCH & ~0x40004000u)

    if (idx >= 16) {
        const int g = xcd, s = idx - 16;
        if (w < 4) {
            const size_t kb = ((size_t)g * 65536 + 61440 + 256 * s + 64 * w) * 256;
            const u32 kw = (u32)(uintptr_t)lds + w * 16384;
            {
                const char* sb = (const char*)kc + kb;
                const u32 vo = (u32)(l >> 4) * 256 + (((l & 15) ^ (4 * (l >> 4))) * 16);
#pragma unroll
                for (int T = 0; T < 4; ++T)
                    asm volatile("s_mov_b32 m0, %5\n s_nop 0\n"
                                 "global_load_lds_dwordx4 %0, %4 nt\n global_load_lds_dwordx4 %1, %4 offset:1024 nt\n"
                                 "global_load_lds_dwordx4 %2, %4 offset:2048 nt\n global_load_lds_dwordx4 %3, %4 offset:3072 nt"
                                 : : "v"(vo), "v"(vo ^ 16u), "v"(vo ^ 32u), "v"(vo ^ 48u), "s"(sb + T * 4096), "s"(kw + T * 4096) : "memory");
            }
            v4u rv[16];
            {
                const char* sb = (const char*)vc + kb;
#pragma unroll
                for (int i = 0; i < 16; ++i) rv[i] = ldnt(sb + i * 1024, (u32)l * 16);
            }
            __builtin_amdgcn_sched_barrier(0);
            TS(1);
            asm volatile("s_waitcnt vmcnt(16)" ::: "memory");
            v4u rk[16];
            {
                const u32 ka = w * 16384 + (l & 3) * 1024 + ((l >> 2) & 3) * 256 + (((((l >> 2) & 3) << 2) | ((l >> 4) ^ (l & 3))) * 16);
#pragma unroll
                for (int t = 0; t < 4; ++t)
#pragma unroll
                    for (int T = 0; T < 4; ++T) rk[4 * T + t] = *(const LAS v4u*)(lds + (ka ^ (t * 64)) + T * 4096);
            }
            TS(2);
            bar();
            TS(3);
            v4u qv[4];
#pragma unroll
            for (int t = 0; t < 4; ++t) qv[t] = *(const LAS v4u*)(lds + L_Q + (l & 3) * 320 + t * 64 + (l >> 4) * 16);
            v4f sc[4];
#pragma unroll
            for (int T = 0; T < 4; ++T) {
                sc[T] = v4f{0.f, 0.f, 0.f, 0.f};
#pragma unroll
                for (int t = 0; t < 4; ++t) sc[T] = mfma32(rk[4 * T + t], qv[t], sc[T]);
            }
            v4u vo0[8], vo1[8];
            perm8(vo0, rv);
            float sv[4];
#pragma unroll
            for (int i = 0; i < 4; ++i) {
                u32 x = asu(sc[0][i]);
                x = __builtin_amdgcn_update_dpp(x, asu(sc[1][i]), QID, 0xf, 0x2, false);
                x = __builtin_amdgcn_update_dpp(x, asu(sc[2][i]), QID, 0xf, 0x4, false);
                x = __builtin_amdgcn_update_dpp(x, asu(sc[3][i]), QID, 0xf, 0x8, false);
                sv[i] = asf(x);
            }
            {
                const u32 a = pk2(sv[0], sv[1]), c = pk2(sv[2], sv[3]);
                const u32 a2 = pk2(lo16(a) * 0.08838834764831845f, hi16(a) * 0.08838834764831845f), c2 = pk2(lo16(c) * 0.08838834764831845f, hi16(c) * 0.08838834764831845f);
                sv[0] = lo16(a2); sv[1] = hi16(a2); sv[2] = lo16(c2); sv[3] = hi16(c2);
            }
            float mx = fmaxf(fmaxf(sv[0], sv[1]), fmaxf(sv[2], sv[3]));
            mx = fmaxf(mx, dpp<ROR(4)>(mx)); mx = fmaxf(mx, dpp<ROR(8)>(mx)); mx = x32max(x16max(mx));
            const float M = __builtin_ceilf(mx * 1.4426950408889634f);
            float e[4];
#pragma unroll
            for (int i = 0; i < 4; ++i) e[i] = __builtin_amdgcn_exp2f(__builtin_fmaf(sv[i], 1.4426950408889634f, -M));
            const u32 p0 = pk2(e[0], e[1]), p1 = pk2(e[2], e[3]);
            const v4u pa0 = {p0, p1, dppu<SHL(4)>(p0), dppu<SHL(4)>(p1)}, pa1 = {dppu<SHL(8)>(p0), dppu<SHL(8)>(p1), dppu<SHL(12)>(p0), dppu<SHL(12)>(p1)};
            float ls = (e[0] + e[1]) + (e[2] + e[3]);
            ls += dpp<ROR(4)>(ls); ls += dpp<ROR(8)>(ls); ls = x32add(x16add(ls));
            if (l < 4) *(LAS v2f*)(lds + L_ST + l * 32 + w * 8) = v2f{M, ls};
            TS(4);
            bar();
            v4f d[8];
#pragma unroll
            for (int j = 0; j < 8; ++j) d[j] = mfma32(pa0, vo0[j], v4f{0.f, 0.f, 0.f, 0.f});
            perm8(vo1, rv + 8);
            TS(5);
#pragma unroll
            for (int j = 0; j < 8; ++j) d[j] = mfma32(pa1, vo1[j], d[j]);
            if (l < 16) {
#pragma unroll
                for (int j = 0; j < 8; ++j) *(LAS v4f*)(lds + w * 16384 + (16 * j + l) * 16) = d[j];
            }
            TS(6);
            bar();
            if (w < 2) {
                v4f o = {0.f, 0.f, 0.f, 0.f};
#pragma unroll
                for (int i = 0; i < 4; ++i) o += *(const LAS v4f*)(lds + i * 16384 + tid * 16) * *(const LAS v4f*)(lds + L_F + i * 16);
                const u32 tag = TAG;
                st16<0>(ws + OFF_XA + g * 32768 + s * 512, (u32)(tid >> 5) * 8192 + (tid & 31) * 16, v4u{pk2(o[0], o[1]), tag, pk2(o[2], o[3]), tag});
            }
            TS(7);
        } else {
            const u32 tag = TAG;
            wait_until(t0, T_QP);
            {
                v4u v0, v1;
                const char* sb = ws + OFF_XQ + g * 2048;
                _Pragma("unroll 1") for (int spin = 0; spin < SPIN_MAX; ++spin) {
                    ld16<0>(v0, sb, l * 16); ld16<1024>(v1, sb, l * 16);
                    wvm<1>(v0); wvm<0>(v1);
                    if (__all((badtag(v0, tag) | badtag(v1, tag)) == 0)) break;
                }
                LAS char* q = lds + L_Q + (l >> 5) * 320 + (l & 31) * 8;
                *(LAS v2u*)q = v2u{v0.x, v0.z};
                *(LAS v2u*)(q + 640) = v2u{v1.x, v1.z};
            }
            TS(3);
            bar();
            bar();
            {
                const v2f st = *(const LAS v2f*)(lds + L_ST + (l & 15) * 8);
                float M = st[0];
                M = fmaxf(M, dpp<QP1>(M)); M = fmaxf(M, dpp<QP2>(M));
                const float f = __builtin_amdgcn_exp2f(st[0] - M);
                if (l < 16) *(LAS float*)(lds + L_F + (l & 3) * 16 + (l >> 2) * 4) = f;
                float ls = st[1] * f;
                ls += dpp<QP1>(ls); ls += dpp<QP2>(ls);
                if (l < 16 && (l & 3) == 0) st16<0>(ws + OFF_XS + (g * 16 + s) * 128, (u32)l * 4, v4u{asu(M), tag, asu(ls), tag});
            }
            TS(5);
            bar();
            if (s < 4) {
                const int f = xcd * 4 + s, k = l & 7, pg = l >> 3;
                const int cl = 8 * (8 * (f & 1) + k) + 2 * ((f & 7) >> 1), col = 128 * (f >> 3) + cl;
                const u32 xr = *(const GAS u32*)(xin + col);
                float ga0, ga1, gb0, gb1, a0, a1;
                {
                    v4u v0, v1;
                    const char* sb = ws + OFF_XM + xcd * 32768 + (f >> 3) * 8192 + 4096;
                    _Pragma("unroll 1") for (int spin = 0; spin < SPIN_MAX; ++spin) {
                        ld16<0>(v0, sb, cl * 8); ld16<3072>(v1, sb, cl * 8);
                        wvm<1>(v0); wvm<0>(v1);
                        if (__all((badtag(v0, tag) | badtag(v1, tag)) == 0)) break;
                        __builtin_amdgcn_s_sleep(4);
                    }
                    ga0 = rbf(asf(v0.x)); ga1 = rbf(asf(v0.z)); gb0 = rbf(asf(v1.x)); gb1 = rbf(asf(v1.z));
                }
                TS(8);
                {
                    v4u v0;
                    const char* sb = ws + OFF_XW + xcd * 32768 + (f >> 3) * 8192;
                    _Pragma("unroll 1") for (int spin = 0; spin < SPIN_MAX; ++spin) {
                        ld16<0>(v0, sb, pg * 1024 + cl * 8);
                        wvm<0>(v0);
                        if (__all(badtag(v0, tag) == 0)) break;
                        __builtin_amdgcn_s_sleep(4);
                    }
                    a0 = asf(v0.x); a1 = asf(v0.z);
                }
                a0 += dpp<ROR(8)>(a0); a1 += dpp<ROR(8)>(a1); a0 = x32add(x16add(a0)); a1 = x32add(x16add(a1));
                const float x20 = rbf(lo16(xr) + rbf(ga0 * rbf(a0))), x21 = rbf(hi16(xr) + rbf(ga1 * rbf(a1)));
                TS(9);
                float y0, y1;
                {
                    const char* sb = ws + OFF_XD + f * 7168 + 3072;
                    _Pragma("unroll 1") for (int spin = 0; spin < SPIN_MAX; ++spin) {
                        v4u v[7];
                        ld16<-3072>(v[0], sb, l * 16); ld16<-2048>(v[1], sb, l * 16); ld16<-1024>(v[2], sb, l * 16); ld16<0>(v[3], sb, l * 16);
                        ld16<1024>(v[4], sb, l * 16); ld16<2048>(v[5], sb, l * 16); ld16<3072>(v[6], sb, l * 16);
                        u32 bad;
                        wvm<6>(v[0]); bad = badtag(v[0], tag); y0 = asf(v[0].x); y1 = asf(v[0].z);
#define FD(i) wvm<6 - i>(v[i]); bad |= badtag(v[i], tag); y0 += asf(v[i].x); y1 += asf(v[i].z);
                        FD(1) FD(2) FD(3) FD(4) FD(5) FD(6)
#undef FD
                        if (__all(bad == 0)) break;
                    }
                }
                TS(10);
                y0 += dpp<ROR(8)>(y0); y1 += dpp<ROR(8)>(y1); y0 = x32add(x16add(y0)); y1 = x32add(x16add(y1));
                const u32 o = pk2(x20 + rbf(gb0 * rbf(y0)), x21 + rbf(gb1 * rbf(y1)));
                if (l < 8) *(GAS u32*)(out + col) = o;
                if (f == 0 && l == 0) __hip_atomic_store((GAS u32*)(ws + OFF_EP), NEXT_EPOCH, __ATOMIC_RELAXED, __HIP_MEMORY_SCOPE_AGENT);
                TS(11);
            }
        }
    } else if (idx >= 8) {
        const int c = idx - 8, g = (xcd + GROT) & 7;
        if (w < 4) {
            v4u r[16];
            const v4u xa = *(const GAS v4u*)(xin + 8 * l);
            const u32 xo = *(const GAS u32*)(xin + 128 * w + 2 * l);
            {
                const char* sb = (const char*)wq + (size_t)(128 * w) * 8192 + g * 1024 + c * 128;
                const u32 vo = (u32)(4 * (l >> 4) + ((l >> 3) & 1)) * 65536u + (l & 7) * 16;
#pragma unroll
                for (int i = 0; i < 16; ++i) r[i] = ldnt(sb + ((i >> 3) * 16 + (i & 7)) * 8192, vo);
            }
            __builtin_amdgcn_sched_barrier(0);
            TS(1);
            float n0, n1;
            {
                float xv[8] = {lo16(xa.x), hi16(xa.x), lo16(xa.y), hi16(xa.y), lo16(xa.z), hi16(xa.z), lo16(xa.w), hi16(xa.w)};
                float sm = ((xv[0] + xv[1]) + (xv[2] + xv[3])) + ((xv[4] + xv[5]) + (xv[6] + xv[7]));
                sm += dpp<QP1>(sm); sm += dpp<QP2>(sm); sm += dpp<ROR(4)>(sm); sm += dpp<ROR(8)>(sm); sm = x32add(x16add(sm));
                const float mean = sm * (1.f / 512.f);
                float v = 0.f;
#pragma unroll
                for (int i = 0; i < 8; ++i) { const float d = xv[i] - mean; v += d * d; }
                v += dpp<QP1>(v); v += dpp<QP2>(v); v += dpp<ROR(4)>(v); v += dpp<ROR(8)>(v); v = x32add(x16add(v));
                const float mb = rbf(mean);
                const float dn = rbf(__builtin_sqrtf(rbf(rbf(v * (1.f / 512.f)) + 1e-5f)));
                n0 = rbf(rbf(lo16(xo) - mb) / dn); n1 = rbf(rbf(hi16(xo) - mb) / dn);
            }
            v4u o0[8], o1[8];
            perm8(o0, r); perm8(o1, r + 8);
            pin8(o0); pin8(o1);
            const u32 tag = TAG;
            TS(2);
            {
                float sa0, sa1, ca0, ca1;
                const char* sb = ws + OFF_XM + xcd * 32768 + w * 8192;
                _Pragma("unroll 1") for (int spin = 0; spin < SPIN_MAX; ++spin) {
                    v4u v[4];
                    ld16<0>(v[0], sb, l * 16); ld16<1024>(v[1], sb, l * 16); ld16<2048>(v[2], sb, l * 16); ld16<3072>(v[3], sb, l * 16);
                    wvm<3>(v[0]); wvm<2>(v[1]);
                    u32 bad = badtag(v[0], tag) | badtag(v[1], tag);
                    sa0 = asf(v[0].x) + asf(v[1].x); sa1 = asf(v[0].z) + asf(v[1].z);
                    wvm<1>(v[2]); wvm<0>(v[3]);
                    bad |= badtag(v[2], tag) | badtag(v[3], tag);
                    ca0 = asf(v[2].x) + asf(v[3].x); ca1 = asf(v[2].z) + asf(v[3].z);
                    if (__all(bad == 0)) break;
                }
                TS(3);
                const u32 sp = pk2(sa0, sa1), cp = pk2(ca0, ca1);
                const u32 c1 = pk2(1.f + lo16(cp), 1.f + hi16(cp));
                const u32 m = pk2(n0 * lo16(c1), n1 * hi16(c1));
                const u32 h = pk2(lo16(m) + lo16(sp), hi16(m) + hi16(sp));
                gemv64(lds + L_R0, v4u{dppu<QB(0)>(h), dppu<QB(1)>(h), dppu<QB(2)>(h), dppu<QB(3)>(h)}, o0, o1, w, l);
            }
            bar();
            TS(4);
            {
                const char* sb = (const char*)wo + (size_t)(g * 512 + 2 * w) * 1024 + c * 128;
                const u32 vo = (u32)(((l >> 3) & 1) * 128 + 32 * (l >> 4)) * 1024u + (l & 7) * 16;
#pragma unroll
                for (int i = 0; i < 16; ++i) r[i] = ldnt(sb + (i >> 3) * 262144 + (i & 3) * 8192 + ((i >> 2) & 1) * 1024, vo);
            }
            __builtin_amdgcn_sched_barrier(0);
            perm8(o0, r); perm8(o1, r + 8);
            pin8(o0); pin8(o1);
            TS(5);
            v4u bt[4]; float rinv;
            {
                v4u v[4];
                const char* sb = ws + OFF_XS + g * 2048;
                const u32 vo = (u32)(l >> 4) * 128 + ((l >> 2) & 3) * 16;
                _Pragma("unroll 1") for (int spin = 0; spin < SPIN_MAX; ++spin) {
                    ld16<0>(v[0], sb, vo); ld16<512>(v[1], sb, vo); ld16<1024>(v[2], sb, vo); ld16<1536>(v[3], sb, vo);
                    wvm<3>(v[0]); wvm<2>(v[1]); wvm<1>(v[2]); wvm<0>(v[3]);
                    if (__all((badtag(v[0], tag) | badtag(v[1], tag) | badtag(v[2], tag) | badtag(v[3], tag)) == 0)) break;
                }
                TS(6);
                float M = fmaxf(fmaxf(asf(v[0].x), asf(v[1].x)), fmaxf(asf(v[2].x), asf(v[3].x)));
                M = x32max(x16max(M));
                const int hd = (l >> 2) & 3;
                const u32 hm = (hd & 1) ? 0xffff0000u : 0x0000ffffu, k0 = hd < 2 ? hm : 0u, k2 = hd < 2 ? 0u : hm;
                float L = 0.f;
#pragma unroll
                for (int a = 0; a < 4; ++a) {
                    const float f = __builtin_amdgcn_exp2f(asf(v[a].x) - M);
                    L += f * asf(v[a].z);
                    const u32 d = pk2(f, f);
                    bt[a] = v4u{d & k0, 0u, d & k2, 0u};
                }
                rinv = 1.f / x32add(x16add(L));
                asm volatile("" : "+v"(bt[0]), "+v"(bt[1]), "+v"(bt[2]), "+v"(bt[3]), "+v"(rinv));
            }
            TS(7);
            {
                v4f d0, d1;
                const char* sb = ws + OFF_XA + g * 32768 + w * 8192 + 4096;
                const u32 vo = (u32)(l >> 4) * 512 + (l & 15) * 16;
                _Pragma("unroll 1") for (int spin = 0; spin < SPIN_MAX; ++spin) {
                    v4u v[8];
                    ld16<-4096>(v[0], sb, vo); ld16<-3840>(v[1], sb, vo); ld16<-2048>(v[2], sb, vo); ld16<-1792>(v[3], sb, vo);
                    ld16<0>(v[4], sb, vo); ld16<256>(v[5], sb, vo); ld16<2048>(v[6], sb, vo); ld16<2304>(v[7], sb, vo);
                    d0 = v4f{0.f, 0.f, 0.f, 0.f}; d1 = d0;
                    u32 bad = 0;
#define FA(a) wvm<7 - 2 * a>(v[2 * a]); d0 = mfma32(v[2 * a], bt[a], d0); bad |= badtag(v[2 * a], tag); \
              wvm<6 - 2 * a>(v[2 * a + 1]); d1 = mfma32(v[2 * a + 1], bt[a], d1); bad |= badtag(v[2 * a + 1], tag);
                    FA(0) FA(1) FA(2) FA(3)
#undef FA
                    if (__all(bad == 0)) break;
                }
                TS(8);
                d0 *= rinv; d1 *= rinv;
                gemv64(lds + L_R1, v4u{pk2(d0[0], d0[1]), pk2(d0[2], d0[3]), pk2(d1[0], d1[1]), pk2(d1[2], d1[3])}, o0, o1, w, l);
            }
            bar();
            TS(9);
        } else {
            const u32 tag = TAG;
            bar();
            {
                const v2f r = redcols<16>(lds + L_R0, l);
                if (l < 32) st8(ws + OFF_XQ + g * 2048 + c * 256, (u32)l * 8, v2u{pk2(r[0], r[1]), tag});
            }
            TS(4);
            bar();
            pub_f32(ws + OFF_XW + (c >> 1) * 8192 + g * 1024 + (c & 1) * 512, redcols<16>(lds + L_R1, l), tag, l);
            TS(9);
        }
    } else {
        const int jm = xcd * 7 + idx;
        const int id = idx < 4 ? xcd * 4 + idx : idx == 7 ? xcd : xcd * 3 + idx + 4;
        const int cb = idx < 4 ? (id >> 1) & 7 : id & 7, slot = idx < 4 ? (id >> 4) * 2 + (id & 1) : 4 + (id >> 3);
        if (w < 4) {
            v4u r0[8], r1[8], r2[8];
            const u32 xo = *(const GAS u32*)(xin + 128 * w + 2 * l);
#define MLP_ISSUE() { \
                const size_t so = (size_t)(128 * w) * 3584 + jm * 64; \
                const u32 vo = (u32)(l >> 2) * 28672u + (l & 3) * 16; \
                _Pragma("unroll") for (int i = 0; i < 8; ++i) r0[i] = ldnt((const char*)wg + so + i * 3584, vo); \
                _Pragma("unroll") for (int i = 0; i < 8; ++i) r1[i] = ldnt((const char*)wu + so + i * 3584, vo); \
                const char* sb = (const char*)wd + (size_t)(32 * jm) * 1024 + 256 * w; \
                const u32 vo2 = (u32)(l >> 4) * 8192u + (l & 15) * 16; \
                _Pragma("unroll") for (int i = 0; i < 8; ++i) r2[i] = ldnt(sb + i * 1024, vo2); \
                __builtin_amdgcn_sched_barrier(0); }
            if (idx < 4) {
                v4u r[8]; u32 cp[1];
                mod_issue<1>(r, cp, wmod, cin, (id >> 4) * 512 + cb * 64, (id & 1) * 256 + 64 * w, l);
                __builtin_amdgcn_sched_barrier(0);
                TS(1);
                mod_compute<1>(lds + L_R0, r, cp, w, l);
                TS(2);
                bar();
                wait_until(t0, T_M);
                MLP_ISSUE();
            } else {
                v4u r[16]; u32 cp[2];
                wait_until(t0, idx < 7 ? T_R : T_R7);
                mod_issue<2>(r, cp, wmod, cin, 1024 + (id >> 3) * 512 + cb * 64, 128 * w, l);
                __builtin_amdgcn_sched_barrier(0);
                if (idx < 7) MLP_ISSUE();
                TS(1);
                mod_compute<2>(lds + L_R0, r, cp, w, l);
                TS(2);
                bar();
            }
            if (idx < 7) {
                v4u og[8], ou[8], od[8];
                perm8(og, r0); perm8(ou, r1); perm8(od, r2);
                pin8(og); pin8(ou); pin8(od);
                const u32 tag = TAG;
                TS(3);
                float ga0, ga1, sb0, sb1, c10, c11;
                {
                    v4u v[3];
                    const char* sb = ws + OFF_XM + xcd * 32768 + w * 8192 + 4096;
                    _Pragma("unroll 1") for (int spin = 0; spin < SPIN_MAX; ++spin) {
                        ld16<0>(v[0], sb, l * 16); ld16<1024>(v[1], sb, l * 16); ld16<2048>(v[2], sb, l * 16);
                        wvm<2>(v[0]); wvm<1>(v[1]); wvm<0>(v[2]);
                        if (__all((badtag(v[0], tag) | badtag(v[1], tag) | badtag(v[2], tag)) == 0)) break;
                        __builtin_amdgcn_s_sleep(4);
                    }
                    ga0 = rbf(asf(v[0].x)); ga1 = rbf(asf(v[0].z)); sb0 = rbf(asf(v[1].x)); sb1 = rbf(asf(v[1].z));
                    c10 = rbf(1.f + rbf(asf(v[2].x))); c11 = rbf(1.f + rbf(asf(v[2].z)));
                    asm volatile("" : "+v"(ga0), "+v"(ga1), "+v"(sb0), "+v"(sb1), "+v"(c10), "+v"(c11));
                }
                TS(4);
                wait_until(t0, T_XW);
                float a0, a1;
                {
                    const char* sb = ws + OFF_XW + xcd * 32768 + w * 8192 + 4096;
                    _Pragma("unroll 1") for (int spin = 0; spin < SPIN_MAX; ++spin) {
                        v4u v[8];
                        ld16<-4096>(v[0], sb, l * 16); ld16<-3072>(v[1], sb, l * 16); ld16<-2048>(v[2], sb, l * 16); ld16<-1024>(v[3], sb, l * 16);
                        ld16<0>(v[4], sb, l * 16); ld16<1024>(v[5], sb, l * 16); ld16<2048>(v[6], sb, l * 16); ld16<3072>(v[7], sb, l * 16);
                        u32 bad;
                        wvm<7>(v[0]); bad = badtag(v[0], tag); a0 = asf(v[0].x); a1 = asf(v[0].z);
#define FW(i) wvm<7 - i>(v[i]); bad |= badtag(v[i], tag); a0 += asf(v[i].x); a1 += asf(v[i].z);
                        FW(1) FW(2) FW(3) FW(4) FW(5) FW(6) FW(7)
#undef FW
                        if (__all(bad == 0)) break;
                    }
                }
                TS(5);
                float x20, x21;
                {
                    const u32 at = pk2(a0, a1);
                    const u32 gm = pk2(ga0 * lo16(at), ga1 * hi16(at));
                    const u32 x2 = pk2(lo16(xo) + lo16(gm), hi16(xo) + hi16(gm));
                    x20 = lo16(x2); x21 = hi16(x2);
                    const float pv = asf(__builtin_amdgcn_readfirstlane(asu(x20)));
                    const float d0 = x20 - pv, d1 = x21 - pv;
                    auto r = __builtin_amdgcn_permlane32_swap(asu(d0 + d1), asu(d0 * d0 + d1 * d1), false, false);
                    float sm = asf(r[0]) + asf(r[1]);
                    sm = x16add(sm);
                    sm += dpp<ROR(8)>(sm); sm += dpp<ROR(4)>(sm); sm += dpp<QP2>(sm); sm += dpp<QP1>(sm);
                    if ((l & 31) == 0) *(LAS v2f*)(lds + L_LN + w * 16 + (l >> 5) * 8) = v2f{sm, pv};
                }
                bar();
                {
                    const v4f t = *(const LAS v4f*)(lds + L_LN + (l & 3) * 16);
                    float a = 128.f * t[1] + t[0];
                    a += dpp<QP1>(a); a += dpp<QP2>(a);
                    const float mean = a * (1.f / 512.f), ee = mean - t[1];
                    float v = t[2] - 2.f * ee * t[0] + 128.f * ee * ee;
                    v += dpp<QP1>(v); v += dpp<QP2>(v);
                    const float mb = rbf(mean);
                    const float dn = rbf(__builtin_amdgcn_sqrtf(rbf(rbf(fmaxf(v, 0.f) * (1.f / 512.f)) + 1e-5f)));
                    const float ri = __builtin_amdgcn_rcpf(dn);
                    const u32 dd = pk2(x20 - mb, x21 - mb);
                    const u32 nn = pk2(lo16(dd) * ri, hi16(dd) * ri);
                    const u32 mm = pk2(lo16(nn) * c10, hi16(nn) * c11);
                    const u32 h = pk2(lo16(mm) + sb0, hi16(mm) + sb1);
                    const v4u av = {dppu<QB(0)>(h), dppu<QB(1)>(h), dppu<QB(2)>(h), dppu<QB(3)>(h)};
                    TS(6);
                    float yg[8], yu[8];
#pragma unroll
                    for (int j = 0; j < 8; ++j) { yg[j] = mfma32(av, og[j], v4f{0.f, 0.f, 0.f, 0.f})[0]; yu[j] = mfma32(av, ou[j], v4f{0.f, 0.f, 0.f, 0.f})[0]; }
                    if (((l >> 2) & 3) == (l >> 4)) {
                        LAS float* q = (LAS float*)(lds + L_RG + (l & 3) * 16 * 80 + (4 * w + (l >> 4)) * 4);
#pragma unroll
                        for (int j = 0; j < 8; ++j) { q[j * 20] = yg[j]; q[(8 + j) * 20] = yu[j]; }
                    }
                }
                bar();
                TS(7);
                {
                    const LAS v4f* q = (const LAS v4f*)(lds + L_RG + l * 80);
                    const v4f t = (q[0] + q[1]) + (q[2] + q[3]);
                    const float a = rbf((t[0] + t[1]) + (t[2] + t[3]));
                    const float uu = dpp<ROR(8)>(a);
                    const float tt = 0.7978845608028654f * (a + 0.044715f * a * a * a);
                    const float th = 1.f - 2.f * __builtin_amdgcn_rcpf(1.f + __builtin_amdgcn_exp2f(tt * 2.885390081777927f));
                    const float hv = rbf(0.5f * a * (1.f + th)) * uu;
                    const u32 hp = pk2(hv, dpp<QP1>(hv));
                    const v4u av = {dppu<NBC(0)>(hp), dppu<NBC(2)>(hp), dppu<NBC(4)>(hp), dppu<NBC(6)>(hp)};
                    u32 y[8];
#pragma unroll
                    for (int j = 0; j < 8; ++j) y[j] = asu(mfma32(av, od[j], v4f{0.f, 0.f, 0.f, 0.f})[0]);
                    u32 y0 = y[0], y1 = y[1];
                    y0 = __builtin_amdgcn_update_dpp(y0, y[2], QID, 0x2, 0xf, false); y1 = __builtin_amdgcn_update_dpp(y1, y[3], QID, 0x2, 0xf, false);
                    y0 = __builtin_amdgcn_update_dpp(y0, y[4], QID, 0x4, 0xf, false); y1 = __builtin_amdgcn_update_dpp(y1, y[5], QID, 0x4, 0xf, false);
                    y0 = __builtin_amdgcn_update_dpp(y0, y[6], QID, 0x8, 0xf, false); y1 = __builtin_amdgcn_update_dpp(y1, y[7], QID, 0x8, 0xf, false);
                    st16<0>(ws + OFF_XD + w * 57344 + jm * 128, (u32)(l >> 3) * 7168u + (l & 7) * 16, v4u{y0, tag, y1, tag});
                }
                TS(8);
            }
        } else {
            const u32 tag = TAG;
            asm volatile("" :: "s"(tag));
            TS(1);
            bar();
            TS(3);
            pub_f32(ws + OFF_XM + (cb >> 1) * 8192 + slot * 1024 + (cb & 1) * 512, redcols<8>(lds + L_R0, l), tag, l);
            TS(2);
            if (idx < 7) { bar(); bar(); }
        }
    }
    TSDUMP();
}

extern "C" void kernel_launch(void* const* d_in, const int* in_sizes, int n_in, void* d_out, int out_size, void* d_ws, size_t ws_size, hipStream_t stream) {
    fused_kernel<<<256, 320, 0, stream>>>((const u16*)d_in[0], (const u16*)d_in[1], (const u16*)d_in[2], (const u16*)d_in[3], (const u16*)d_in[4],
        (const u16*)d_in[6], (const u16*)d_in[7], (const u16*)d_in[8], (const u16*)d_in[9], (const u16*)d_in[10], (u16*)d_out, (char*)d_ws);
}
```

```cpp
#define ENC 0
#include <hip/hip_runtime.h>
#include <stdint.h>

typedef unsigned u32;
typedef unsigned short u16;
typedef u32 v4u __attribute__((ext_vector_type(4)));
typedef u32 v2u __attribute__((ext_vector_type(2)));
typedef float v4f __attribute__((ext_vector_type(4)));
typedef float v2f __attribute__((ext_vector_type(2)));
typedef __bf16 v8bf __attribute__((ext_vector_type(8)));
typedef __bf16 v2bf __attribute__((ext_vector_type(2)));
#define GAS __attribute__((address_space(1)))
#define LAS __attribute__((address_space(3)))

#define T_M 260
#define T_R 150
#define T_R7 150
#define T_QP 150
#define T_XW 0
#define GROT 1

constexpr u32 OFF_EP = 0;
constexpr u32 OFF_XQ = 16384;
constexpr u32 OFF_XS = 32768;
constexpr u32 OFF_XM = 65536;
constexpr u32 OFF_XA = 327680;
constexpr u32 OFF_XW = 589824;
constexpr u32 OFF_XD = 851968;
constexpr u32 OFF_DBG = 2u << 20;
constexpr int SPIN_MAX = 1 << 16;
constexpr u32 L_Q = 65536, L_ST = 67072, L_F = 67200, L_SIZE = 86016;
constexpr u32 L_R0 = 0, L_R1 = 4096, L_LN = 8192, L_RG = 8448;

__device__ __forceinline__ float asf(u32 v) { return __uint_as_float(v); }
__device__ __forceinline__ u32 asu(float v) { return __float_as_uint(v); }
__device__ __forceinline__ float lo16(u32 v) { return asf(v << 16); }
__device__ __forceinline__ float hi16(u32 v) { return asf(v & 0xffff0000u); }
__device__ __forceinline__ u32 pk2(float a, float b) { v2f t = {a, b}; v2bf r = __builtin_convertvector(t, v2bf); return __builtin_bit_cast(u32, r); }
__device__ __forceinline__ float rbf(float a) { return lo16(pk2(a, a)); }
__device__ __forceinline__ v4u ldnt(const char* sb, u32 vo) { return __builtin_nontemporal_load((const GAS v4u*)(sb + vo)); }
template <int IMM> __device__ __forceinline__ void st16(char* sb, u32 vo, v4u v) { asm volatile("global_store_dwordx4 %0, %1, %2 offset:%3 sc1\n s_nop 1" : : "v"(vo), "v"(v), "s"(sb), "n"(IMM) : "memory"); }
__device__ __forceinline__ void st8(char* sb, u32 vo, v2u v) { asm volatile("global_store_dwordx2 %0, %1, %2 sc1\n s_nop 1" : : "v"(vo), "v"(v), "s"(sb) : "memory"); }
template <int IMM> __device__ __forceinline__ void ld16(v4u& v, const char* sb, u32 vo) { asm volatile("global_load_dwordx4 %0, %1, %2 offset:%3 sc1" : "=v"(v) : "v"(vo), "s"(sb), "n"(IMM) : "memory"); }
template <int N> __device__ __forceinline__ void wvm(v4u& v) { asm volatile("s_waitcnt vmcnt(%1)" : "+v"(v) : "n"(N) : "memory"); }
__device__ __forceinline__ void bar() { asm volatile("s_waitcnt lgkmcnt(0)\n s_barrier" ::: "memory"); }
template <int C> __device__ __forceinline__ u32 dppu(u32 x) { return __builtin_amdgcn_update_dpp(0, x, C, 0xf, 0xf, true); }
template <int C> __device__ __forceinline__ float dpp(float x) { return asf(dppu<C>(asu(x))); }
#define QP1 0xB1
#define QP2 0x4E
#define QID 0xE4
#define QB(n) ((n) * 0x55)
#define SHL(n) (0x100 + (n))
#define ROR(n) (0x120 + (n))
#define NBC(n) (0x150 + (n))
__device__ __forceinline__ float x16add(float x) { auto r = __builtin_amdgcn_permlane16_swap(asu(x), asu(x), false, false); return asf(r[0]) + asf(r[1]); }
__device__ __forceinline__ float x32add(float x) { auto r = __builtin_amdgcn_permlane32_swap(asu(x), asu(x), false, false); return asf(r[0]) + asf(r[1]); }
__device__ __forceinline__ float x16max(float x) { auto r = __builtin_amdgcn_permlane16_swap(asu(x), asu(x), false, false); return fmaxf(asf(r[0]), asf(r[1])); }
__device__ __forceinline__ float x32max(float x) { auto r = __builtin_amdgcn_permlane32_swap(asu(x), asu(x), false, false); return fmaxf(asf(r[0]), asf(r[1])); }
__device__ __forceinline__ u32 badtag(const v4u& v, u32 tag) { return (v.y ^ tag) | (v.w ^ tag); }
__device__ __forceinline__ v4f mfma32(v4u a, v4u b, v4f c) {
    return __builtin_amdgcn_mfma_f32_16x16x32_bf16(__builtin_bit_cast(v8bf, a), __builtin_bit_cast(v8bf, b), c, 0, 0, 0);
}
__device__ __forceinline__ void perm8(v4u (&op)[8], const v4u* r) {
#pragma unroll
    for (int p = 0; p < 4; ++p)
#pragma unroll
        for (int j = 0; j < 8; ++j) op[j][p] = __builtin_amdgcn_perm(r[2 * p + 1][j >> 1], r[2 * p][j >> 1], (j & 1) ? 0x07060302u : 0x05040100u);
}
__device__ __forceinline__ void pin8(v4u (&op)[8]) {
    asm volatile("" : "+v"(op[0]), "+v"(op[1]), "+v"(op[2]), "+v"(op[3]), "+v"(op[4]), "+v"(op[5]), "+v"(op[6]), "+v"(op[7]));
}
__device__ __forceinline__ float silu_f(float v) { return v * __builtin_amdgcn_rcpf(1.f + __expf(-v)); }
__device__ __forceinline__ u32 now() { return (u32)__builtin_amdgcn_s_memrealtime(); }
__device__ __forceinline__ void wait_until(u32 t0, u32 d) { if (d) while (now() - t0 < d) __builtin_amdgcn_s_sleep(2); }
template <int NS> __device__ __forceinline__ v2f redcols(LAS char* red, int l) {
    const volatile LAS v2f* q = (const volatile LAS v2f*)(red + (l >> 5) * (NS / 2) * 256 + (l & 31) * 8);
    v2f a[NS / 2];
#pragma unroll
    for (int i = 0; i < NS / 2; ++i) a[i] = q[i * 32];
#pragma unroll
    for (int s = NS / 4; s > 0; s >>= 1)
#pragma unroll
        for (int i = 0; i < s; ++i) a[i] += a[i + s];
    return v2f{x32add(a[0][0]), x32add(a[0][1])};
}
__device__ __forceinline__ void gemv64(LAS char* red, v4u a, const v4u (&o0)[8], const v4u (&o1)[8], int w, int l) {
    float y[8];
#pragma unroll
    for (int j = 0; j < 8; ++j) {
        const v4f d0 = mfma32(a, o0[j], v4f{0.f, 0.f, 0.f, 0.f}), d1 = mfma32(a, o1[j], v4f{0.f, 0.f, 0.f, 0.f});
        y[j] = asf(__builtin_amdgcn_update_dpp(asu(d0[0]), asu(d1[0]), QID, 0xc, 0xf, false));
    }
    if (((l >> 3) & 1) == ((l >> 4) & 1)) {
        LAS v4f* q = (LAS v4f*)(red + (4 * w + (l >> 4)) * 256 + (l & 7) * 32);
        q[0] = v4f{y[0], y[1], y[2], y[3]}; q[1] = v4f{y[4], y[5], y[6], y[7]};
    }
}
template <int NB> __device__ __forceinline__ void mod_issue(v4u (&r)[8 * NB], u32 (&cp)[NB], const u16* wmod, const u16* cin, int col0, int row0, int l) {
#pragma unroll
    for (int k = 0; k < NB; ++k) cp[k] = *(const GAS u32*)(cin + row0 + 64 * k + 8 * (l >> 3) + 2 * (l & 3));
    const char* sb = (const char*)wmod + (size_t)row0 * 6144 + col0 * 2;
    const u32 vo = (u32)(l >> 3) * 49152u + (l & 7) * 16;
#pragma unroll
    for (int i = 0; i < 8 * NB; ++i) r[i] = ldnt(sb + ((i >> 3) * 64 + (i & 7)) * 6144, vo);
}
template <int NB> __device__ __forceinline__ void mod_compute(LAS char* red, const v4u (&r)[8 * NB], const u32 (&cp)[NB], int w, int l) {
    const u32 hm = (l & 1) ? 0xffff0000u : 0x0000ffffu;
    const int e2 = (l & 7) >> 1;
    const u32 m0 = e2 == 0 ? hm : 0u, m1 = e2 == 1 ? hm : 0u, m2 = e2 == 2 ? hm : 0u, m3 = e2 == 3 ? hm : 0u;
    v4f acc = {0.f, 0.f, 0.f, 0.f};
#pragma unroll
    for (int k = 0; k < NB; ++k) {
        const float s0 = silu_f(lo16(cp[k])), s1 = silu_f(hi16(cp[k]));
        const u32 d0 = pk2(s0, s0), d1 = pk2(s1, s1);
#define MODT(t) { const u32 x = dppu<QB((t) >> 1)>(((t) & 1) ? d1 : d0); acc = mfma32(v4u{x & m0, x & m1, x & m2, x & m3}, r[8 * k + (t)], acc); }
        MODT(0) MODT(1) MODT(2) MODT(3) MODT(4) MODT(5) MODT(6) MODT(7)
#undef MODT
    }
    if (((l >> 3) & 1) == (l >> 5)) *(LAS v4f*)(red + (2 * w + (l >> 5)) * 256 + (l & 7) * 32 + ((l >> 4) & 1) * 16) = acc;
}
__device__ __forceinline__ void pub_f32(char* sb, v2f r, u32 tag, int l) {
    const v4u v = {asu(r[0]), tag, asu(r[1]), tag};
    const u32 vo = (u32)(l >> 5) * 32768u + (l & 31) * 16;
    st16<0>(sb, vo, v); st16<0>(sb + 65536, vo, v); st16<0>(sb + 131072, vo, v); st16<0>(sb + 196608, vo, v);
}

#define TS(i) do { if (ENC == (i)) { __builtin_amdgcn_sched_barrier(0); te = now(); __builtin_amdgcn_sched_barrier(0); } } while (0)
#define TSDUMP()

__global__ void __launch_bounds__(320) fused_kernel(const u16* __restrict__ xin, const u16* __restrict__ kc, const u16* __restrict__ vc,
        const u16* __restrict__ wq, const u16* __restrict__ wo, const u16* __restrict__ wg, const u16* __restrict__ wu,
        const u16* __restrict__ wd, const u16* __restrict__ cin, const u16* __restrict__ wmod, u16* __restrict__ out, char* __restrict__ ws) {
    __shared__ __attribute__((aligned(16))) char lds_[L_SIZE];
    LAS char* const lds = (LAS char*)lds_;
    const int b = blockIdx.x, tid = threadIdx.x, l = tid & 63;
    const int w = __builtin_amdgcn_readfirstlane(tid >> 6);
    const int xcd = b & 7, idx = b >> 3;
    const u32 t0 = now();
    u32 te = t0;
    const u32 epoch = __hip_atomic_load((const GAS u32*)(ws + OFF_EP), __ATOMIC_RELAXED, __HIP_MEMORY_SCOPE_AGENT);
#define NEXT_EPOCH (__builtin_amdgcn_readfirstlane(epoch) * 1664525u + 1013904223u)
#define TAG (NEXT_EPOCH & ~0x40004000u)

    if (idx >= 16) {
        const int g = xcd, s = idx - 16;
        if (w < 4) {
            const size_t kb = ((size_t)g * 65536 + 61440 + 256 * s + 64 * w) * 256;
            const u32 kw = (u32)(uintptr_t)lds + w * 16384;
            {
                const char* sb = (const char*)kc + kb;
                const u32 vo = (u32)(l >> 4) * 256 + (((l & 15) ^ (4 * (l >> 4))) * 16);
#pragma unroll
                for (int T = 0; T < 4; ++T)
                    asm volatile("s_mov_b32 m0, %5\n s_nop 0\n"
                                 "global_load_lds_dwordx4 %0, %4 nt\n global_load_lds_dwordx4 %1, %4 offset:1024 nt\n"
                                 "global_load_lds_dwordx4 %2, %4 offset:2048 nt\n global_load_lds_dwordx4 %3, %4 offset:3072 nt"
                                 : : "v"(vo), "v"(vo ^ 16u), "v"(vo ^ 32u), "v"(vo ^ 48u), "s"(sb + T * 4096), "s"(kw + T * 4096) : "memory");
            }
            v4u rv[16];
            {
                const char* sb = (const char*)vc + kb;
#pragma unroll
                for (int i = 0; i < 16; ++i) rv[i] = ldnt(sb + i * 1024, (u32)l * 16);
            }
            __builtin_amdgcn_sched_barrier(0);
            TS(1);
            asm volatile("s_waitcnt vmcnt(16)" ::: "memory");
            v4u rk[16];
            {
                const u32 ka = w * 16384 + (l & 3) * 1024 + ((l >> 2) & 3) * 256 + (((((l >> 2) & 3) << 2) | ((l >> 4) ^ (l & 3))) * 16);
#pragma unroll
                for (int t = 0; t < 4; ++t)
#pragma unroll
                    for (int T = 0; T < 4; ++T) rk[4 * T + t] = *(const LAS v4u*)(lds + (ka ^ (t * 64)) + T * 4096);
            }
            TS(2);
            bar();
            TS(3);
            v4u qv[4];
#pragma unroll
            for (int t = 0; t < 4; ++t) qv[t] = *(const LAS v4u*)(lds + L_Q + (l & 3) * 320 + t * 64 + (l >> 4) * 16);
            v4f sc[4];
#pragma unroll
            for (int T = 0; T < 4; ++T) {
                sc[T] = v4f{0.f, 0.f, 0.f, 0.f};
#pragma unroll
                for (int t = 0; t < 4; ++t) sc[T] = mfma32(rk[4 * T + t], qv[t], sc[T]);
            }
            v4u vo0[8], vo1[8];
            perm8(vo0, rv);
            float sv[4];
#pragma unroll
            for (int i = 0; i < 4; ++i) {
                u32 x = asu(sc[0][i]);
                x = __builtin_amdgcn_update_dpp(x, asu(sc[1][i]), QID, 0xf, 0x2, false);
                x = __builtin_amdgcn_update_dpp(x, asu(sc[2][i]), QID, 0xf, 0x4, false);
                x = __builtin_amdgcn_update_dpp(x, asu(sc[3][i]), QID, 0xf, 0x8, false);
                sv[i] = asf(x);
            }
            {
                const u32 a = pk2(sv[0], sv[1]), c = pk2(sv[2], sv[3]);
                const u32 a2 = pk2(lo16(a) * 0.08838834764831845f, hi16(a) * 0.08838834764831845f), c2 = pk2(lo16(c) * 0.08838834764831845f, hi16(c) * 0.08838834764831845f);
                sv[0] = lo16(a2); sv[1] = hi16(a2); sv[2] = lo16(c2); sv[3] = hi16(c2);
            }
            float mx = fmaxf(fmaxf(sv[0], sv[1]), fmaxf(sv[2], sv[3]));
            mx = fmaxf(mx, dpp<ROR(4)>(mx)); mx = fmaxf(mx, dpp<ROR(8)>(mx)); mx = x32max(x16max(mx));
            const float M = __builtin_ceilf(mx * 1.4426950408889634f);
            float e[4];
#pragma unroll
            for (int i = 0; i < 4; ++i) e[i] = __builtin_amdgcn_exp2f(__builtin_fmaf(sv[i], 1.4426950408889634f, -M));
            const u32 p0 = pk2(e[0], e[1]), p1 = pk2(e[2], e[3]);
            const v4u pa0 = {p0, p1, dppu<SHL(4)>(p0), dppu<SHL(4)>(p1)}, pa1 = {dppu<SHL(8)>(p0), dppu<SHL(8)>(p1), dppu<SHL(12)>(p0), dppu<SHL(12)>(p1)};
            float ls = (e[0] + e[1]) + (e[2] + e[3]);
            ls += dpp<ROR(4)>(ls); ls += dpp<ROR(8)>(ls); ls = x32add(x16add(ls));
            if (l < 4) *(LAS v2f*)(lds + L_ST + l * 32 + w * 8) = v2f{M, ls};
            TS(4);
            bar();
            v4f d[8];
#pragma unroll
            for (int j = 0; j < 8; ++j) d[j] = mfma32(pa0, vo0[j], v4f{0.f, 0.f, 0.f, 0.f});
            perm8(vo1, rv + 8);
            TS(5);
#pragma unroll
            for (int j = 0; j < 8; ++j) d[j] = mfma32(pa1, vo1[j], d[j]);
            if (l < 16) {
#pragma unroll
                for (int j = 0; j < 8; ++j) *(LAS v4f*)(lds + w * 16384 + (16 * j + l) * 16) = d[j];
            }
            TS(6);
            bar();
            if (w < 2) {
                v4f o = {0.f, 0.f, 0.f, 0.f};
#pragma unroll
                for (int i = 0; i < 4; ++i) o += *(const LAS v4f*)(lds + i * 16384 + tid * 16) * *(const LAS v4f*)(lds + L_F + i * 16);
                const u32 tag = TAG;
                st16<0>(ws + OFF_XA + g * 32768 + s * 512, (u32)(tid >> 5) * 8192 + (tid & 31) * 16, v4u{pk2(o[0], o[1]), tag, pk2(o[2], o[3]), tag});
            }
            TS(7);
        } else {
            const u32 tag = TAG;
            wait_until(t0, T_QP);
            {
                v4u v0, v1;
                const char* sb = ws + OFF_XQ + g * 2048;
                _Pragma("unroll 1") for (int spin = 0; spin < SPIN_MAX; ++spin) {
                    ld16<0>(v0, sb, l * 16); ld16<1024>(v1, sb, l * 16);
                    wvm<1>(v0); wvm<0>(v1);
                    if (__all((badtag(v0, tag) | badtag(v1, tag)) == 0)) break;
                }
                LAS char* q = lds + L_Q + (l >> 5) * 320 + (l & 31) * 8;
                *(LAS v2u*)q = v2u{v0.x, v0.z};
                *(LAS v2u*)(q + 640) = v2u{v1.x, v1.z};
            }
            TS(3);
            bar();
            bar();
            {
                const v2f st = *(const LAS v2f*)(lds + L_ST + (l & 15) * 8);
                float M = st[0];
                M = fmaxf(M, dpp<QP1>(M)); M = fmaxf(M, dpp<QP2>(M));
                const float f = __builtin_amdgcn_exp2f(st[0] - M);
                if (l < 16) *(LAS float*)(lds + L_F + (l & 3) * 16 + (l >> 2) * 4) = f;
                float ls = st[1] * f;
                ls += dpp<QP1>(ls); ls += dpp<QP2>(ls);
                if (l < 16 && (l & 3) == 0) st16<0>(ws + OFF_XS + (g * 16 + s) * 128, (u32)l * 4, v4u{asu(M), tag, asu(ls), tag});
            }
            TS(5);
            bar();
            if (s < 4) {
                const int f = xcd * 4 + s, k = l & 7, pg = l >> 3;
                const int cl = 8 * (8 * (f & 1) + k) + 2 * ((f & 7) >> 1), col = 128 * (f >> 3) + cl;
                const u32 xr = *(const GAS u32*)(xin + col);
                float ga0, ga1, gb0, gb1, a0, a1;
                {
                    v4u v0, v1;
                    const char* sb = ws + OFF_XM + xcd * 32768 + (f >> 3) * 8192 + 4096;
                    _Pragma("unroll 1") for (int spin = 0; spin < SPIN_MAX; ++spin) {
                        ld16<0>(v0, sb, cl * 8); ld16<3072>(v1, sb, cl * 8);
                        wvm<1>(v0); wvm<0>(v1);
                        if (__all((badtag(v0, tag) | badtag(v1, tag)) == 0)) break;
                        __builtin_amdgcn_s_sleep(4);
                    }
                    ga0 = rbf(asf(v0.x)); ga1 = rbf(asf(v0.z)); gb0 = rbf(asf(v1.x)); gb1 = rbf(asf(v1.z));
                }
                TS(8);
                {
                    v4u v0;
                    const char* sb = ws + OFF_XW + xcd * 32768 + (f >> 3) * 8192;
                    _Pragma("unroll 1") for (int spin = 0; spin < SPIN_MAX; ++spin) {
                        ld16<0>(v0, sb, pg * 1024 + cl * 8);
                        wvm<0>(v0);
                        if (__all(badtag(v0, tag) == 0)) break;
                        __builtin_amdgcn_s_sleep(4);
                    }
                    a0 = asf(v0.x); a1 = asf(v0.z);
                }
                a0 += dpp<ROR(8)>(a0); a1 += dpp<ROR(8)>(a1); a0 = x32add(x16add(a0)); a1 = x32add(x16add(a1));
                const float x20 = rbf(lo16(xr) + rbf(ga0 * rbf(a0))), x21 = rbf(hi16(xr) + rbf(ga1 * rbf(a1)));
                TS(9);
                float y0, y1;
                {
                    const char* sb = ws + OFF_XD + f * 7168 + 3072;
                    _Pragma("unroll 1") for (int spin = 0; spin < SPIN_MAX; ++spin) {
                        v4u v[7];
                        ld16<-3072>(v[0], sb, l * 16); ld16<-2048>(v[1], sb, l * 16); ld16<-1024>(v[2], sb, l * 16); ld16<0>(v[3], sb, l * 16);
                        ld16<1024>(v[4], sb, l * 16); ld16<2048>(v[5], sb, l * 16); ld16<3072>(v[6], sb, l * 16);
                        u32 bad;
                        wvm<6>(v[0]); bad = badtag(v[0], tag); y0 = asf(v[0].x); y1 = asf(v[0].z);
#define FD(i) wvm<6 - i>(v[i]); bad |= badtag(v[i], tag); y0 += asf(v[i].x); y1 += asf(v[i].z);
                        FD(1) FD(2) FD(3) FD(4) FD(5) FD(6)
#undef FD
                        if (__all(bad == 0)) break;
                    }
                }
                TS(10);
                y0 += dpp<ROR(8)>(y0); y1 += dpp<ROR(8)>(y1); y0 = x32add(x16add(y0)); y1 = x32add(x16add(y1));
                const u32 o = pk2(x20 + rbf(gb0 * rbf(y0)), x21 + rbf(gb1 * rbf(y1)));
                if (l < 8) __hip_atomic_store((GAS u32*)(out + col), o, __ATOMIC_RELAXED, __HIP_MEMORY_SCOPE_AGENT);
                if (f == 0 && l == 0) __hip_atomic_store((GAS u32*)(ws + OFF_EP), NEXT_EPOCH, __ATOMIC_RELAXED, __HIP_MEMORY_SCOPE_AGENT);
                TS(11);
                if (f == 0) { while (now() - t0 < 4000 + (te - t0)) __builtin_amdgcn_s_sleep(1); }
            }
        }
    } else if (idx >= 8) {
        const int c = idx - 8, g = (xcd + GROT) & 7;
        if (w < 4) {
            v4u r[16];
            const v4u xa = *(const GAS v4u*)(xin + 8 * l);
            const u32 xo = *(const GAS u32*)(xin + 128 * w + 2 * l);
            {
                const char* sb = (const char*)wq + (size_t)(128 * w) * 8192 + g * 1024 + c * 128;
                const u32 vo = (u32)(4 * (l >> 4) + ((l >> 3) & 1)) * 65536u + (l & 7) * 16;
#pragma unroll
                for (int i = 0; i < 16; ++i) r[i] = ldnt(sb + ((i >> 3) * 16 + (i & 7)) * 8192, vo);
            }
            __builtin_amdgcn_sched_barrier(0);
            TS(1);
            float n0, n1;
            {
                float xv[8] = {lo16(xa.x), hi16(xa.x), lo16(xa.y), hi16(xa.y), lo16(xa.z), hi16(xa.z), lo16(xa.w), hi16(xa.w)};
                float sm = ((xv[0] + xv[1]) + (xv[2] + xv[3])) + ((xv[4] + xv[5]) + (xv[6] + xv[7]));
                sm += dpp<QP1>(sm); sm += dpp<QP2>(sm); sm += dpp<ROR(4)>(sm); sm += dpp<ROR(8)>(sm); sm = x32add(x16add(sm));
                const float mean = sm * (1.f / 512.f);
                float v = 0.f;
#pragma unroll
                for (int i = 0; i < 8; ++i) { const float d = xv[i] - mean; v += d * d; }
                v += dpp<QP1>(v); v += dpp<QP2>(v); v += dpp<ROR(4)>(v); v += dpp<ROR(8)>(v); v = x32add(x16add(v));
                const float mb = rbf(mean);
                const float dn = rbf(__builtin_sqrtf(rbf(rbf(v * (1.f / 512.f)) + 1e-5f)));
                n0 = rbf(rbf(lo16(xo) - mb) / dn); n1 = rbf(rbf(hi16(xo) - mb) / dn);
            }
            v4u o0[8], o1[8];
            perm8(o0, r); perm8(o1, r + 8);
            pin8(o0); pin8(o1);
            const u32 tag = TAG;
            TS(2);
            {
                float sa0, sa1, ca0, ca1;
                const char* sb = ws + OFF_XM + xcd * 32768 + w * 8192;
                _Pragma("unroll 1") for (int spin = 0; spin < SPIN_MAX; ++spin) {
                    v4u v[4];
                    ld16<0>(v[0], sb, l * 16); ld16<1024>(v[1], sb, l * 16); ld16<2048>(v[2], sb, l * 16); ld16<3072>(v[3], sb, l * 16);
                    wvm<3>(v[0]); wvm<2>(v[1]);
                    u32 bad = badtag(v[0], tag) | badtag(v[1], tag);
                    sa0 = asf(v[0].x) + asf(v[1].x); sa1 = asf(v[0].z) + asf(v[1].z);
                    wvm<1>(v[2]); wvm<0>(v[3]);
                    bad |= badtag(v[2], tag) | badtag(v[3], tag);
                    ca0 = asf(v[2].x) + asf(v[3].x); ca1 = asf(v[2].z) + asf(v[3].z);
                    if (__all(bad == 0)) break;
                }
                TS(3);
                const u32 sp = pk2(sa0, sa1), cp = pk2(ca0, ca1);
                const u32 c1 = pk2(1.f + lo16(cp), 1.f + hi16(cp));
                const u32 m = pk2(n0 * lo16(c1), n1 * hi16(c1));
                const u32 h = pk2(lo16(m) + lo16(sp), hi16(m) + hi16(sp));
                gemv64(lds + L_R0, v4u{dppu<QB(0)>(h), dppu<QB(1)>(h), dppu<QB(2)>(h), dppu<QB(3)>(h)}, o0, o1, w, l);
            }
            bar();
            TS(4);
            {
                const char* sb = (const char*)wo + (size_t)(g * 512 + 2 * w) * 1024 + c * 128;
                const u32 vo = (u32)(((l >> 3) & 1) * 128 + 32 * (l >> 4)) * 1024u + (l & 7) * 16;
#pragma unroll
                for (int i = 0; i < 16; ++i) r[i] = ldnt(sb + (i >> 3) * 262144 + (i & 3) * 8192 + ((i >> 2) & 1) * 1024, vo);
            }
            __builtin_amdgcn_sched_barrier(0);
            perm8(o0, r); perm8(o1, r + 8);
            pin8(o0); pin8(o1);
            TS(5);
            v4u bt[4]; float rinv;
            {
                v4u v[4];
                const char* sb = ws + OFF_XS + g * 2048;
                const u32 vo = (u32)(l >> 4) * 128 + ((l >> 2) & 3) * 16;
                _Pragma("unroll 1") for (int spin = 0; spin < SPIN_MAX; ++spin) {
                    ld16<0>(v[0], sb, vo); ld16<512>(v[1], sb, vo); ld16<1024>(v[2], sb, vo); ld16<1536>(v[3], sb, vo);
                    wvm<3>(v[0]); wvm<2>(v[1]); wvm<1>(v[2]); wvm<0>(v[3]);
                    if (__all((badtag(v[0], tag) | badtag(v[1], tag) | badtag(v[2], tag) | badtag(v[3], tag)) == 0)) break;
                }
                TS(6);
                float M = fmaxf(fmaxf(asf(v[0].x), asf(v[1].x)), fmaxf(asf(v[2].x), asf(v[3].x)));
                M = x32max(x16max(M));
                const int hd = (l >> 2) & 3;
                const u32 hm = (hd & 1) ? 0xffff0000u : 0x0000ffffu, k0 = hd < 2 ? hm : 0u, k2 = hd < 2 ? 0u : hm;
                float L = 0.f;
#pragma unroll
                for (int a = 0; a < 4; ++a) {
                    const float f = __builtin_amdgcn_exp2f(asf(v[a].x) - M);
                    L += f * asf(v[a].z);
                    const u32 d = pk2(f, f);
                    bt[a] = v4u{d & k0, 0u, d & k2, 0u};
                }
                rinv = 1.f / x32add(x16add(L));
                asm volatile("" : "+v"(bt[0]), "+v"(bt[1]), "+v"(bt[2]), "+v"(bt[3]), "+v"(rinv));
            }
            TS(7);
            {
                v4f d0, d1;
                const char* sb = ws + OFF_XA + g * 32768 + w * 8192 + 4096;
                const u32 vo = (u32)(l >> 4) * 512 + (l & 15) * 16;
                _Pragma("unroll 1") for (int spin = 0; spin < SPIN_MAX; ++spin) {
                    v4u v[8];
                    ld16<-4096>(v[0], sb, vo); ld16<-3840>(v[1], sb, vo); ld16<-2048>(v[2], sb, vo); ld16<-1792>(v[3], sb, vo);
                    ld16<0>(v[4], sb, vo); ld16<256>(v[5], sb, vo); ld16<2048>(v[6], sb, vo); ld16<2304>(v[7], sb, vo);
                    d0 = v4f{0.f, 0.f, 0.f, 0.f}; d1 = d0;
                    u32 bad = 0;
#define FA(a) wvm<7 - 2 * a>(v[2 * a]); d0 = mfma32(v[2 * a], bt[a], d0); bad |= badtag(v[2 * a], tag); \
              wvm<6 - 2 * a>(v[2 * a + 1]); d1 = mfma32(v[2 * a + 1], bt[a], d1); bad |= badtag(v[2 * a + 1], tag);
                    FA(0) FA(1) FA(2) FA(3)
#undef FA
                    if (__all(bad == 0)) break;
                }
                TS(8);
                d0 *= rinv; d1 *= rinv;
                gemv64(lds + L_R1, v4u{pk2(d0[0], d0[1]), pk2(d0[2], d0[3]), pk2(d1[0], d1[1]), pk2(d1[2], d1[3])}, o0, o1, w, l);
            }
            bar();
            TS(9);
        } else {
            const u32 tag = TAG;
            bar();
            {
                const v2f r = redcols<16>(lds + L_R0, l);
                if (l < 32) st8(ws + OFF_XQ + g * 2048 + c * 256, (u32)l * 8, v2u{pk2(r[0], r[1]), tag});
            }
            TS(4);
            bar();
            pub_f32(ws + OFF_XW + (c >> 1) * 8192 + g * 1024 + (c & 1) * 512, redcols<16>(lds + L_R1, l), tag, l);
            TS(9);
        }
    } else {
        const int jm = xcd * 7 + idx;
        const int id = idx < 4 ? xcd * 4 + idx : idx == 7 ? xcd : xcd * 3 + idx + 4;
        const int cb = idx < 4 ? (id >> 1) & 7 : id & 7, slot = idx < 4 ? (id >> 4) * 2 + (id & 1) : 4 + (id >> 3);
        if (w < 4) {
            v4u r0[8], r1[8], r2[8];
            const u32 xo = *(const GAS u32*)(xin + 128 * w + 2 * l);
#define MLP_ISSUE() { \
                const size_t so = (size_t)(128 * w) * 3584 + jm * 64; \
                const u32 vo = (u32)(l >> 2) * 28672u + (l & 3) * 16; \
                _Pragma("unroll") for (int i = 0; i < 8; ++i) r0[i] = ldnt((const char*)wg + so + i * 3584, vo); \
                _Pragma("unroll") for (int i = 0; i < 8; ++i) r1[i] = ldnt((const char*)wu + so + i * 3584, vo); \
                const char* sb = (const char*)wd + (size_t)(32 * jm) * 1024 + 256 * w; \
                const u32 vo2 = (u32)(l >> 4) * 8192u + (l & 15) * 16; \
                _Pragma("unroll") for (int i = 0; i < 8; ++i) r2[i] = ldnt(sb + i * 1024, vo2); \
                __builtin_amdgcn_sched_barrier(0); }
            if (idx < 4) {
                v4u r[8]; u32 cp[1];
                mod_issue<1>(r, cp, wmod, cin, (id >> 4) * 512 + cb * 64, (id & 1) * 256 + 64 * w, l);
                __builtin_amdgcn_sched_barrier(0);
                TS(1);
                mod_compute<1>(lds + L_R0, r, cp, w, l);
                TS(2);
                bar();
                wait_until(t0, T_M);
                MLP_ISSUE();
            } else {
                v4u r[16]; u32 cp[2];
                wait_until(t0, idx < 7 ? T_R : T_R7);
                mod_issue<2>(r, cp, wmod, cin, 1024 + (id >> 3) * 512 + cb * 64, 128 * w, l);
                __builtin_amdgcn_sched_barrier(0);
                if (idx < 7) MLP_ISSUE();
                TS(1);
                mod_compute<2>(lds + L_R0, r, cp, w, l);
                TS(2);
                bar();
            }
            if (idx < 7) {
                v4u og[8], ou[8], od[8];
                perm8(og, r0); perm8(ou, r1); perm8(od, r2);
                pin8(og); pin8(ou); pin8(od);
                const u32 tag = TAG;
                TS(3);
                float ga0, ga1, sb0, sb1, c10, c11;
                {
                    v4u v[3];
                    const char* sb = ws + OFF_XM + xcd * 32768 + w * 8192 + 4096;
                    _Pragma("unroll 1") for (int spin = 0; spin < SPIN_MAX; ++spin) {
                        ld16<0>(v[0], sb, l * 16); ld16<1024>(v[1], sb, l * 16); ld16<2048>(v[2], sb, l * 16);
                        wvm<2>(v[0]); wvm<1>(v[1]); wvm<0>(v[2]);
                        if (__all((badtag(v[0], tag) | badtag(v[1], tag) | badtag(v[2], tag)) == 0)) break;
                        __builtin_amdgcn_s_sleep(4);
                    }
                    ga0 = rbf(asf(v[0].x)); ga1 = rbf(asf(v[0].z)); sb0 = rbf(asf(v[1].x)); sb1 = rbf(asf(v[1].z));
                    c10 = rbf(1.f + rbf(asf(v[2].x))); c11 = rbf(1.f + rbf(asf(v[2].z)));
                    asm volatile("" : "+v"(ga0), "+v"(ga1), "+v"(sb0), "+v"(sb1), "+v"(c10), "+v"(c11));
                }
                TS(4);
                wait_until(t0, T_XW);
                float a0, a1;
                {
                    const char* sb = ws + OFF_XW + xcd * 32768 + w * 8192 + 4096;
                    _Pragma("unroll 1") for (int spin = 0; spin < SPIN_MAX; ++spin) {
                        v4u v[8];
                        ld16<-4096>(v[0], sb, l * 16); ld16<-3072>(v[1], sb, l * 16); ld16<-2048>(v[2], sb, l * 16); ld16<-1024>(v[3], sb, l * 16);
                        ld16<0>(v[4], sb, l * 16); ld16<1024>(v[5], sb, l * 16); ld16<2048>(v[6], sb, l * 16); ld16<3072>(v[7], sb, l * 16);
                        u32 bad;
                        wvm<7>(v[0]); bad = badtag(v[0], tag); a0 = asf(v[0].x); a1 = asf(v[0].z);
#define FW(i) wvm<7 - i>(v[i]); bad |= badtag(v[i], tag); a0 += asf(v[i].x); a1 += asf(v[i].z);
                        FW(1) FW(2) FW(3) FW(4) FW(5) FW(6) FW(7)
#undef FW
                        if (__all(bad == 0)) break;
                    }
                }
                TS(5);
                float x20, x21;
                {
                    const u32 at = pk2(a0, a1);
                    const u32 gm = pk2(ga0 * lo16(at), ga1 * hi16(at));
                    const u32 x2 = pk2(lo16(xo) + lo16(gm), hi16(xo) + hi16(gm));
                    x20 = lo16(x2); x21 = hi16(x2);
                    const float pv = asf(__builtin_amdgcn_readfirstlane(asu(x20)));
                    const float d0 = x20 - pv, d1 = x21 - pv;
                    auto r = __builtin_amdgcn_permlane32_swap(asu(d0 + d1), asu(d0 * d0 + d1 * d1), false, false);
                    float sm = asf(r[0]) + asf(r[1]);
                    sm = x16add(sm);
                    sm += dpp<ROR(8)>(sm); sm += dpp<ROR(4)>(sm); sm += dpp<QP2>(sm); sm += dpp<QP1>(sm);
                    if ((l & 31) == 0) *(LAS v2f*)(lds + L_LN + w * 16 + (l >> 5) * 8) = v2f{sm, pv};
                }
                bar();
                {
                    const v4f t = *(const LAS v4f*)(lds + L_LN + (l & 3) * 16);
                    float a = 128.f * t[1] + t[0];
                    a += dpp<QP1>(a); a += dpp<QP2>(a);
                    const float mean = a * (1.f / 512.f), ee = mean - t[1];
                    float v = t[2] - 2.f * ee * t[0] + 128.f * ee * ee;
                    v += dpp<QP1>(v); v += dpp<QP2>(v);
                    const float mb = rbf(mean);
                    const float dn = rbf(__builtin_amdgcn_sqrtf(rbf(rbf(fmaxf(v, 0.f) * (1.f / 512.f)) + 1e-5f)));
                    const float ri = __builtin_amdgcn_rcpf(dn);
                    const u32 dd = pk2(x20 - mb, x21 - mb);
                    const u32 nn = pk2(lo16(dd) * ri, hi16(dd) * ri);
                    const u32 mm = pk2(lo16(nn) * c10, hi16(nn) * c11);
                    const u32 h = pk2(lo16(mm) + sb0, hi16(mm) + sb1);
                    const v4u av = {dppu<QB(0)>(h), dppu<QB(1)>(h), dppu<QB(2)>(h), dppu<QB(3)>(h)};
                    TS(6);
                    float yg[8], yu[8];
#pragma unroll
                    for (int j = 0; j < 8; ++j) { yg[j] = mfma32(av, og[j], v4f{0.f, 0.f, 0.f, 0.f})[0]; yu[j] = mfma32(av, ou[j], v4f{0.f, 0.f, 0.f, 0.f})[0]; }
                    if (((l >> 2) & 3) == (l >> 4)) {
                        LAS float* q = (LAS float*)(lds + L_RG + (l & 3) * 16 * 80 + (4 * w + (l >> 4)) * 4);
#pragma unroll
                        for (int j = 0; j < 8; ++j) { q[j * 20] = yg[j]; q[(8 + j) * 20] = yu[j]; }
                    }
                }
                bar();
                TS(7);
                {
                    const LAS v4f* q = (const LAS v4f*)(lds + L_RG + l * 80);
                    const v4f t = (q[0] + q[1]) + (q[2] + q[3]);
                    const float a = rbf((t[0] + t[1]) + (t[2] + t[3]));
                    const float uu = dpp<ROR(8)>(a);
                    const float tt = 0.7978845608028654f * (a + 0.044715f * a * a * a);
                    const float th = 1.f - 2.f * __builtin_amdgcn_rcpf(1.f + __builtin_amdgcn_exp2f(tt * 2.885390081777927f));
                    const float hv = rbf(0.5f * a * (1.f + th)) * uu;
                    const u32 hp = pk2(hv, dpp<QP1>(hv));
                    const v4u av = {dppu<NBC(0)>(hp), dppu<NBC(2)>(hp), dppu<NBC(4)>(hp), dppu<NBC(6)>(hp)};
                    u32 y[8];
#pragma unroll
                    for (int j = 0; j < 8; ++j) y[j] = asu(mfma32(av, od[j], v4f{0.f, 0.f, 0.f, 0.f})[0]);
                    u32 y0 = y[0], y1 = y[1];
                    y0 = __builtin_amdgcn_update_dpp(y0, y[2], QID, 0x2, 0xf, false); y1 = __builtin_amdgcn_update_dpp(y1, y[3], QID, 0x2, 0xf, false);
                    y0 = __builtin_amdgcn_update_dpp(y0, y[4], QID, 0x4, 0xf, false); y1 = __builtin_amdgcn_update_dpp(y1, y[5], QID, 0x4, 0xf, false);
                    y0 = __builtin_amdgcn_update_dpp(y0, y[6], QID, 0x8, 0xf, false); y1 = __builtin_amdgcn_update_dpp(y1, y[7], QID, 0x8, 0xf, false);
                    st16<0>(ws + OFF_XD + w * 57344 + jm * 128, (u32)(l >> 3) * 7168u + (l & 7) * 16, v4u{y0, tag, y1, tag});
                }
                TS(8);
            }
        } else {
            const u32 tag = TAG;
            asm volatile("" :: "s"(tag));
            TS(1);
            bar();
            TS(3);
            pub_f32(ws + OFF_XM + (cb >> 1) * 8192 + slot * 1024 + (cb & 1) * 512, redcols<8>(lds + L_R0, l), tag, l);
            TS(2);
            if (idx < 7) { bar(); bar(); }
        }
    }
    TSDUMP();
}

extern "C" void kernel_launch(void* const* d_in, const int* in_sizes, int n_in, void* d_out, int out_size, void* d_ws, size_t ws_size, hipStream_t stream) {
    fused_kernel<<<256, 320, 0, stream>>>((const u16*)d_in[0], (const u16*)d_in[1], (const u16*)d_in[2], (const u16*)d_in[3], (const u16*)d_in[4],
        (const u16*)d_in[6], (const u16*)d_in[7], (const u16*)d_in[8], (const u16*)d_in[9], (const u16*)d_in[10], (u16*)d_out, (char*)d_ws);
}
```

```cpp
#define TW 7
#define T_Q 60
#define PFM 2
#define SLM 0
#define SKD 6
#define T_K 10
#define T_K2 30
#define T_PQ 10
#define T_QP 410
#define D_WO 60
#include <hip/hip_runtime.h>
#include <stdint.h>

typedef unsigned u32;
typedef unsigned short u16;
typedef u32 v4u __attribute__((ext_vector_type(4)));
typedef u32 v2u __attribute__((ext_vector_type(2)));
typedef float v4f __attribute__((ext_vector_type(4)));
typedef float v2f __attribute__((ext_vector_type(2)));
typedef __bf16 v8bf __attribute__((ext_vector_type(8)));
typedef __bf16 v2bf __attribute__((ext_vector_type(2)));
#define GAS __attribute__((address_space(1)))
#define LAS __attribute__((address_space(3)))

#define KNOB_DEFAULTS
#define WIN 6
#define PFC 4
#define GROT 1
#define D_PW 0
#define D_M 0
#define POL 2
#define POLH 2
#define CALB 64
#define EPS 2
#define T_PA 280
#define QSL 0
#define T_OP 0
#define T_TW 60
#define ME 0
#define T_ME 300
#define HSP 0
#define XE 0
#define T_L 450
#define T_DRY 0
#define CMAP 0
#define QR 0
#define QD 8
#define XPRE 0
#define KM 132
#define PTA 0
#define GMIX 0
#define NEAR 0
#define OPK 0
#define OOM 0
#define AGRP(x, s) (NEAR ? (((x) & 3) | (((s) >> 3) << 2)) : GMIX == 0 ? (x) : GMIX == 1 ? ((x) + (s)) & 7 : GMIX == 2 ? ((x) & 4) | (((x) + (s)) & 3) : ((x) + ((s) >> 1)) & 7)
#define KAP 2
#define T_MP 0
#define PV0 1
#define HMAP 0
#define RMAP 0
#define T_R 0
constexpr int KM0 = KM;
#define KP8 u16* __restrict__ out
#define KPAR
#define KA8 (u16*)d_out
#define KARG
#define KA_8(C) C "{s[86:87]}"(out)
#define KA_X(C)
#define KA_L
#define KX 0

constexpr u32 OFF_EP = 0;
constexpr u32 OFF_PL = 4096 + 512;
constexpr u32 OFF_CAL = 16384;
constexpr u32 OFF_XQ = 1966080 + 512;
constexpr u32 OFF_XS = OFF_XQ + 4096;
constexpr u32 OFF_XH = 131072 + 512;
constexpr u32 OFF_XA = 393216 + 512;
constexpr u32 OFF_XW = 917504 + 512;
constexpr u32 OFF_XD = 1441792 + 512;
constexpr u32 OFF_XR = 2097152 + 512;
constexpr u32 OFF_DBG = 4u << 20;
constexpr int SPIN_MAX = 1 << 16;
constexpr u32 L_Q = 65536, L_ST = 67072, L_F = 67200, L_PL = 67328, L_PF = 69632, L_RR = 70656, L_SIZE = 86016;
constexpr u32 L_R0 = 0, L_R1 = 4096, L_LN = 8192, L_RG = 8448;

__device__ __forceinline__ float asf(u32 v) { return __uint_as_float(v); }
__device__ __forceinline__ u32 asu(float v) { return __float_as_uint(v); }
__device__ __forceinline__ float lo16(u32 v) { return asf(v << 16); }
__device__ __forceinline__ float hi16(u32 v) { return asf(v & 0xffff0000u); }
__device__ __forceinline__ u32 pk2(float a, float b) { v2f t = {a, b}; v2bf r = __builtin_convertvector(t, v2bf); return __builtin_bit_cast(u32, r); }
__device__ __forceinline__ float rbf(float a) { return asf(pk2(0.f, a)); }
__device__ __forceinline__ v4u ldnt(const char* sb, u32 vo) { return __builtin_nontemporal_load((const GAS v4u*)(sb + vo)); }
template <int IMM> __device__ __forceinline__ void st16(char* sb, u32 vo, v4u v) { asm volatile("global_store_dwordx4 %0, %1, %2 offset:%3 sc1\n s_nop 1" : : "v"(vo), "v"(v), "s"(sb), "n"(IMM) : "memory"); }
__device__ __forceinline__ void st8(char* sb, u32 vo, v2u v) { asm volatile("global_store_dwordx2 %0, %1, %2 sc1\n s_nop 1" : : "v"(vo), "v"(v), "s"(sb) : "memory"); }
template <int IMM> __device__ __forceinline__ void ld16(v4u& v, const char* sb, u32 vo) { asm volatile("global_load_dwordx4 %0, %1, %2 offset:%3 sc1" : "=v"(v) : "v"(vo), "s"(sb), "n"(IMM) : "memory"); }
template <int IMM> __device__ __forceinline__ void ldn(v4u& v, const char* sb, u32 vo) { asm volatile("global_load_dwordx4 %0, %1, %2 offset:%3 nt" : "=v"(v) : "v"(vo), "s"(sb), "n"(IMM) : "memory"); }
template <int IMM> __device__ __forceinline__ void ldn64(v4u& v, const char* p) { asm volatile("global_load_dwordx4 %0, %1, off offset:%2 nt" : "=v"(v) : "v"(p), "n"(IMM) : "memory"); }
template <int N> __device__ __forceinline__ void wvm8(v4u* v) {
    asm volatile("s_waitcnt vmcnt(%8)" : "+v"(v[0]), "+v"(v[1]), "+v"(v[2]), "+v"(v[3]), "+v"(v[4]), "+v"(v[5]), "+v"(v[6]), "+v"(v[7]) : "n"(N) : "memory");
}
template <int N> __device__ __forceinline__ void wvm(v4u& v) { asm volatile("s_waitcnt vmcnt(%1)" : "+v"(v) : "n"(N) : "memory"); }
__device__ __forceinline__ void pf2(const char* sb, u32 v0, u32 v1, u32 dump) {
    asm volatile("s_mov_b32 m0, %3\n s_nop 0\n global_load_lds_dword %0, %2" "\n global_load_lds_dword %1, %2" : : "v"(v0), "v"(v1), "s"(sb), "s"(dump) : "memory");
}
__device__ __forceinline__ void pf1(const char* sb, u32 v0, u32 dump) {
    asm volatile("s_mov_b32 m0, %2\n s_nop 0\n global_load_lds_dword %0, %1" : : "v"(v0), "s"(sb), "s"(dump) : "memory");
}
__device__ __forceinline__ u32 slow_line(const void* p) { return (3u - ((u32)(uintptr_t)p >> 7)) & 7u; }
__device__ __forceinline__ void bar() { asm volatile("s_waitcnt lgkmcnt(0)\n s_barrier" ::: "memory"); }
template <int C> __device__ __forceinline__ u32 dppu(u32 x) { return __builtin_amdgcn_update_dpp(0, x, C, 0xf, 0xf, true); }
template <int C> __device__ __forceinline__ float dpp(float x) { return asf(dppu<C>(asu(x))); }
#define QP1 0xB1
#define QP2 0x4E
#define QID 0xE4
#define QB(n) ((n) * 0x55)
#define SHL(n) (0x100 + (n))
#define ROR(n) (0x120 + (n))
#define NBC(n) (0x150 + (n))
__device__ __forceinline__ float x16add(float x) { auto r = __builtin_amdgcn_permlane16_swap(asu(x), asu(x), false, false); return asf(r[0]) + asf(r[1]); }
__device__ __forceinline__ float x32add(float x) { auto r = __builtin_amdgcn_permlane32_swap(asu(x), asu(x), false, false); return asf(r[0]) + asf(r[1]); }
__device__ __forceinline__ float x16max(float x) { auto r = __builtin_amdgcn_permlane16_swap(asu(x), asu(x), false, false); return fmaxf(asf(r[0]), asf(r[1])); }
__device__ __forceinline__ float x32max(float x) { auto r = __builtin_amdgcn_permlane32_swap(asu(x), asu(x), false, false); return fmaxf(asf(r[0]), asf(r[1])); }
__device__ __forceinline__ u32 badtag(const v4u& v, u32 tag) { return (v.y ^ tag) | (v.w ^ tag); }
__device__ __forceinline__ v4f mfma32(v4u a, v4u b, v4f c) {
    return __builtin_amdgcn_mfma_f32_16x16x32_bf16(__builtin_bit_cast(v8bf, a), __builtin_bit_cast(v8bf, b), c, 0, 0, 0);
}
__device__ __forceinline__ void perm8(v4u (&op)[8], const v4u* r) {
#pragma unroll
    for (int p = 0; p < 4; ++p)
#pragma unroll
        for (int j = 0; j < 8; ++j) op[j][p] = __builtin_amdgcn_perm(r[2 * p + 1][j >> 1], r[2 * p][j >> 1], (j & 1) ? 0x07060302u : 0x05040100u);
}
__device__ __forceinline__ void pin8(v4u (&op)[8]) {
    asm volatile("" : "+v"(op[0]), "+v"(op[1]), "+v"(op[2]), "+v"(op[3]), "+v"(op[4]), "+v"(op[5]), "+v"(op[6]), "+v"(op[7]));
}
__device__ __forceinline__ float silu_f(float v) { return v * __builtin_amdgcn_rcpf(1.f + __expf(-v)); }
__device__ __forceinline__ int sel(bool c, int a, int b) { const int m = -(int)c; return (a & m) | (b & ~m); }
__device__ __forceinline__ u32 now() { return (u32)__builtin_amdgcn_s_memrealtime(); }
__device__ __forceinline__ void wait_until(u32 t0, u32 d) { if (d) while (now() - t0 < d) __builtin_amdgcn_s_sleep(2); }
template <int NS> __device__ __forceinline__ v2f redcols(LAS char* red, int l) {
    const volatile LAS v2f* q = (const volatile LAS v2f*)(red + (l >> 5) * (NS / 2) * 256 + (l & 31) * 8);
    v2f a[NS / 2];
#pragma unroll
    for (int i = 0; i < NS / 2; ++i) a[i] = q[i * 32];
#pragma unroll
    for (int s = NS / 4; s > 0; s >>= 1)
#pragma unroll
        for (int i = 0; i < s; ++i) a[i] += a[i + s];
    return v2f{x32add(a[0][0]), x32add(a[0][1])};
}
__device__ __forceinline__ void gemv64(LAS char* red, v4u a, const v4u (&o0)[8], const v4u (&o1)[8], int w, int l) {
    float y[8];
#pragma unroll
    for (int j = 0; j < 8; ++j) {
        const v4f d0 = mfma32(a, o0[j], v4f{0.f, 0.f, 0.f, 0.f}), d1 = mfma32(a, o1[j], v4f{0.f, 0.f, 0.f, 0.f});
        y[j] = asf(__builtin_amdgcn_update_dpp(asu(d0[0]), asu(d1[0]), QID, 0xc, 0xf, false));
    }
    if (((l >> 3) & 1) == ((l >> 4) & 1)) {
        LAS v4f* q = (LAS v4f*)(red + (4 * w + (l >> 4)) * 256 + (l & 7) * 32);
        q[0] = v4f{y[0], y[1], y[2], y[3]}; q[1] = v4f{y[4], y[5], y[6], y[7]};
    }
}
__device__ __forceinline__ v4f mod_compute(v4f acc, const v4u* r, u32 cp, int l) {
    const u32 hm = (l & 1) ? 0xffff0000u : 0x0000ffffu;
    const int e2 = (l & 7) >> 1;
    const u32 m0 = e2 == 0 ? hm : 0u, m1 = e2 == 1 ? hm : 0u, m2 = e2 == 2 ? hm : 0u, m3 = e2 == 3 ? hm : 0u;
    const float s0 = silu_f(lo16(cp)), s1 = silu_f(hi16(cp));
    const u32 d0 = pk2(s0, s0), d1 = pk2(s1, s1);
#define MODT(t) { const u32 x = dppu<QB((t) >> 1)>(((t) & 1) ? d1 : d0); acc = mfma32(v4u{x & m0, x & m1, x & m2, x & m3}, r[t], acc); }
    MODT(0) MODT(1) MODT(2) MODT(3) MODT(4) MODT(5) MODT(6) MODT(7)
#undef MODT
    return acc;
}
template <u32 RS> __device__ __forceinline__ void pub_f32(char* sb, v2f r, u32 tag, int l) {
    const v4u v = {asu(r[0]), tag, asu(r[1]), tag};
    const u32 vo = (u32)(l >> 5) * 8192u + (l & 31) * 16;
    st16<0>(sb, vo, v); st16<0>(sb + RS, vo, v); st16<0>(sb + 2 * RS, vo, v); st16<0>(sb + 3 * RS, vo, v);
}

#define PTH 0
#define PSL ""
#define STR_(x) #x
#define STR(x) STR_(x)
#define PV4(B, i) "v[" #B "+4*" #i ":" #B "+4*" #i "+3]"
#define PV1(B, i, c) "v[" #B "+4*" #i "+" #c "]"
#define PM(i) "s[40+2*(" #i "):41+2*(" #i ")]"
#define P_INI(B, i) "v_not_b32 " PV1(B, i, 1) ", %[tag]\n"
#define P_CHK(B, i) "v_bitop3_b32 %[t], " PV1(B, i, 1) ", " PV1(B, i, 3) ", %[tag] bitop3:0x7e\n v_cmp_ne_u32 " PM(i) ", 0, %[t]\n"
#define P_OR(i) "s_or_b64 s[56:57], s[56:57], " PM(i) "\n"
#define P_LX(B, i, S, OFF) "s_mov_b64 exec, " PM(i) "\n s_cbranch_execz .Lps" #i "_%=\n global_load_dwordx4 " PV4(B, i) ", %[vo], %[" #S "] offset:" #OFF " sc1\n.Lps" #i "_%=:\n"
#define P_LD(B, i, OFF) P_LX(B, i, sb, OFF)
#define P_LE(B, i, OFF) P_LX(B, i, sc, OFF)
#define P_HEAD "s_nop 4\n s_mov_b64 s[58:59], exec\n s_mov_b32 s60, 0x10000\n"
#define P_TOP ".Lpt_%=:\n s_waitcnt vmcnt(" STR(PTH) ")\n"
#define PT7 7
#define PT5 0
#define PTS 0
#define P_TOP7 ".Lpt_%=:\n s_waitcnt vmcnt(" STR(PT7) ")\n"
#define P_MID "s_cbranch_scc0 .Lpd_%=\n"
#define P_OR01 "s_or_b64 s[56:57], " PM(0) ", " PM(1) "\n"
#define P_TAIL "s_mov_b64 exec, s[58:59]\n" PSL " s_sub_u32 s60, s60, 1\n s_cbranch_scc0 .Lpt_%=\n s_waitcnt vmcnt(0)\n.Lpd_%=:\n s_nop 7\n s_nop 7"
#define P_IN(vo_, sb_, tag_) [vo] "v"(vo_), [sb] "s"(sb_), [tag] "s"(tag_)
#define P_CLOB "s40", "s41", "s42", "s43", "s44", "s45", "s46", "s47", "s48", "s49", "s50", "s51", "s52", "s53", "s54", "s55", "s56", "s57", "s58", "s59", "s60", "scc", "memory"
#define P_REG(C, B, i) C "{v[" #B "+" #i "]}"
#define POLL2(v, vo_, sb_, tag_, O0, O1) { u32 t_; asm volatile(P_HEAD P_INI(128, 0) P_INI(128, 1) P_TOP P_CHK(128, 0) P_CHK(128, 1) P_OR01 P_MID \
    P_LD(128, 0, O0) P_LD(128, 1, O1) P_TAIL : "=&{v[128:131]}"(v[0]), "=&{v[132:135]}"(v[1]), [t] "=&v"(t_) : P_IN(vo_, sb_, tag_) : P_CLOB); }
#define QN 6
#define POLL2P(v, vo_, sb_, tag_, O0, O1) { u32 t_; asm volatile(P_HEAD "s_mov_b32 s61, 0\n" P_INI(128, 0) P_INI(128, 1) ".Lpt_%=:\n" P_CHK(128, 0) P_CHK(128, 1) P_OR01 "s_cbranch_scc0 .Lpd_%=\n" \
    "s_sub_u32 s61, s61, 1\n s_cbranch_scc1 .Lpi_%=\n s_sleep 1\n s_branch .Lpt_%=\n.Lpi_%=:\n s_mov_b32 s61, " STR(QN) "\n s_waitcnt vmcnt(16)\n" \
    P_LD(128, 0, O0) P_LD(128, 1, O1) P_TAIL : "=&{v[128:131]}"(v[0]), "=&{v[132:135]}"(v[1]), [t] "=&v"(t_) : P_IN(vo_, sb_, tag_) : "s61", P_CLOB); }
#define DRAIN2(v) asm volatile("s_waitcnt vmcnt(0)" : : "{v[128:131]}"(v[0]), "{v[132:135]}"(v[1]) : "memory")
#define POLL4(v, vo_, sb_, tag_, O0, O1, O2, O3) { u32 t_; asm volatile(P_HEAD P_INI(160, 0) P_INI(160, 1) P_INI(160, 2) P_INI(160, 3) P_TOP \
    P_CHK(160, 0) P_CHK(160, 1) P_CHK(160, 2) P_CHK(160, 3) P_OR01 P_OR(2) P_OR(3) P_MID \
    P_LD(160, 0, O0) P_LD(160, 1, O1) P_LD(160, 2, O2) P_LD(160, 3, O3) P_TAIL \
    : "=&{v[160:163]}"(v[0]), "=&{v[164:167]}"(v[1]), "=&{v[168:171]}"(v[2]), "=&{v[172:175]}"(v[3]), [t] "=&v"(t_) : P_IN(vo_, sb_, tag_) : P_CLOB); }
#define POLL4E(v, vo_, sb_, tag_, O0, O1, O2, O3) { u32 t_; asm volatile(P_HEAD "s_waitcnt vmcnt(0)\n" P_TOP \
    P_CHK(160, 0) P_CHK(160, 1) P_CHK(160, 2) P_CHK(160, 3) P_OR01 P_OR(2) P_OR(3) P_MID \
    P_LD(160, 0, O0) P_LD(160, 1, O1) P_LD(160, 2, O2) P_LD(160, 3, O3) P_TAIL \
    : "+{v[160:163]}"(v[0]), "+{v[164:167]}"(v[1]), "+{v[168:171]}"(v[2]), "+{v[172:175]}"(v[3]), [t] "=&v"(t_) : P_IN(vo_, sb_, tag_) : P_CLOB); }
#define KEEP4(v) "{v[160:163]}"(v[0]), "{v[164:167]}"(v[1]), "{v[168:171]}"(v[2]), "{v[172:175]}"(v[3])
#define DRAIN4(v) asm volatile("s_waitcnt vmcnt(0)" : : KEEP4(v) : "memory")
#define P_OUT7(v) "=&{v[128:131]}"(v[0]), "=&{v[132:135]}"(v[1]), "=&{v[136:139]}"(v[2]), "=&{v[140:143]}"(v[3]), "=&{v[144:147]}"(v[4]), "=&{v[148:151]}"(v[5]), "=&{v[152:155]}"(v[6])
#define KEEP7(v) "{v[128:131]}"(v[0]), "{v[132:135]}"(v[1]), "{v[136:139]}"(v[2]), "{v[140:143]}"(v[3]), "{v[144:147]}"(v[4]), "{v[148:151]}"(v[5]), "{v[152:155]}"(v[6])
#define POLL7(v, vo_, sb_, tag_, O0, O1, O2, O3, O4, O5, O6) { u32 t_; asm volatile(P_HEAD P_INI(128, 0) P_INI(128, 1) P_INI(128, 2) P_INI(128, 3) P_INI(128, 4) P_INI(128, 5) P_INI(128, 6) P_TOP7 \
    P_CHK(128, 0) P_CHK(128, 1) P_CHK(128, 2) P_CHK(128, 3) P_CHK(128, 4) P_CHK(128, 5) P_CHK(128, 6) P_OR01 P_OR(2) P_OR(3) P_OR(4) P_OR(5) P_OR(6) P_MID \
    P_LD(128, 0, O0) P_LD(128, 1, O1) P_LD(128, 2, O2) P_LD(128, 3, O3) P_LE(128, 4, O4) P_LE(128, 5, O5) P_LE(128, 6, O6) P_TAIL \
    : P_OUT7(v), [t] "=&v"(t_) : P_IN(vo_, sb_, tag_), [sc] "s"(sb_ + 16384) : P_CLOB); }
#define POLL8(v, vo_, sb_, tag_, O0, O1, O2, O3, O4, O5, O6, O7) { u32 t_; asm volatile(P_HEAD P_INI(128, 0) P_INI(128, 1) P_INI(128, 2) P_INI(128, 3) P_INI(128, 4) P_INI(128, 5) P_INI(128, 6) P_INI(128, 7) P_TOP \
    P_CHK(128, 0) P_CHK(128, 1) P_CHK(128, 2) P_CHK(128, 3) P_CHK(128, 4) P_CHK(128, 5) P_CHK(128, 6) P_CHK(128, 7) P_OR01 P_OR(2) P_OR(3) P_OR(4) P_OR(5) P_OR(6) P_OR(7) P_MID \
    P_LD(128, 0, O0) P_LD(128, 1, O1) P_LD(128, 2, O2) P_LD(128, 3, O3) P_LE(128, 4, O4) P_LE(128, 5, O5) P_LE(128, 6, O6) P_LE(128, 7, O7) P_TAIL \
    : P_OUT7(v), "=&{v[156:159]}"(v[7]), [t] "=&v"(t_) : P_IN(vo_, sb_, tag_), [sc] "s"(sb_ + 16384) : P_CLOB); }
#define KEEP8(v) KEEP7(v), "{v[156:159]}"(v[7])
#define PT1 0
#define QM(i) "s[62+2*" #i ":63+2*" #i "]"
#define Q_CHK(i) "v_bitop3_b32 %[t], " PV1(160, i, 1) ", " PV1(160, i, 3) ", %[tag] bitop3:0x7e\n v_cmp_ne_u32 " QM(i) ", 0, %[t]\n"
#define Q_LD(i, OFF) "s_mov_b64 exec, " QM(i) "\n s_cbranch_execz .Lqs" #i "_%=\n global_load_dwordx4 " PV4(160, i) ", %[vq], %[sq] offset:" #OFF " sc1\n.Lqs" #i "_%=:\n"
#define Q_GATE(a, i, j) "s_andn2_b64 " PM(i) ", " PM(i) ", " QM(a) "\n s_andn2_b64 " PM(j) ", " PM(j) ", " QM(a) "\n"
#define Q_OR(i) "s_or_b64 s[56:57], s[56:57], " QM(i) "\n"
#define Q_OR01 "s_mov_b64 exec, s[58:59]\n s_or_b64 s[56:57], " QM(0) ", " QM(1) "\n"
#define Q_TAIL "s_cbranch_scc0 .Lpd_%=\n s_sub_u32 s60, s60, 1\n s_cbranch_scc0 .Lpt_%=\n s_waitcnt vmcnt(0)\n.Lpd_%=:\n s_nop 7\n s_nop 7"
#define P_INI8 P_INI(128, 0) P_INI(128, 1) P_INI(128, 2) P_INI(128, 3) P_INI(128, 4) P_INI(128, 5) P_INI(128, 6) P_INI(128, 7)
#define P_CHK8 P_CHK(128, 0) P_CHK(128, 1) P_CHK(128, 2) P_CHK(128, 3) P_CHK(128, 4) P_CHK(128, 5) P_CHK(128, 6) P_CHK(128, 7)
#define P_LD8(O0, O1, O2, O3) P_LD(128, 0, O0) P_LD(128, 1, O1) P_LD(128, 2, O2) P_LD(128, 3, O3) P_LE(128, 4, O0) P_LE(128, 5, O1) P_LE(128, 6, O2) P_LE(128, 7, O3)
#define Q_CLOB "s62", "s63", "s64", "s65", "s66", "s67", "s68", "s69", P_CLOB
#define Q_NEW(a) "s_mov_b64 " PM(a) ", " QM(a) "\n" Q_CHK(a) "s_andn2_b64 " PM(a) ", " PM(a) ", " QM(a) "\n"
#define Q_LA(a, i, j, S, O0, O1) "s_mov_b64 exec, " PM(a) "\n s_cbranch_execz .Lqa" #a "_%=\n global_load_dwordx4 " PV4(128, i) ", %[vo], %[" #S "] offset:" #O0 " sc1\n" \
    "global_load_dwordx4 " PV4(128, j) ", %[vo], %[" #S "] offset:" #O1 " sc1\n.Lqa" #a "_%=:\n"
#define Q_ALL(a) "s_mov_b64 " QM(a) ", exec\n s_mov_b64 " PM(a) ", 0\n"
#undef Q_LA
#define Q_LA(a, i, j, S, O0, O1)
#define Q_L0(i, OFF) "global_load_dwordx4 " PV4(160, i) ", %[vq], %[sq] offset:" #OFF " sc1\n"
#define Q_L1(i, S, OFF) "global_load_dwordx4 " PV4(128, i) ", %[vo], %[" #S "] offset:" #OFF " sc1\n"
#define Q_PRE(O0, O1, O2, O3)
#define POLL4G8(u, vq_, sq_, U0, U1, U2, U3, v, vo_, sb_, tag_, O0, O1, O2, O3) { u32 t_; asm volatile(P_HEAD P_INI8 P_INI(160, 0) P_INI(160, 1) P_INI(160, 2) P_INI(160, 3) \
    Q_ALL(0) Q_ALL(1) Q_ALL(2) Q_ALL(3) Q_L0(0, U0) Q_L0(1, U1) Q_L0(2, U2) Q_L0(3, U3) Q_PRE(O0, O1, O2, O3) ".Lpt_%=:\n s_waitcnt vmcnt(" STR(PT1) ")\n" Q_NEW(0) Q_NEW(1) Q_NEW(2) Q_NEW(3) \
    Q_LD(0, U0) Q_LD(1, U1) Q_LD(2, U2) Q_LD(3, U3) Q_LA(0, 0, 1, sb, O0, O1) Q_LA(1, 2, 3, sb, O2, O3) Q_LA(2, 4, 5, sc, O0, O1) Q_LA(3, 6, 7, sc, O2, O3) Q_OR01 Q_OR(2) Q_OR(3) \
    "s_cbranch_scc0 .Lpd_%=\n s_sub_u32 s60, s60, 1\n s_cbranch_scc0 .Lpt_%=\n s_waitcnt vmcnt(0)\n.Lpd_%=:\n" P_CHK8 P_LD8(O0, O1, O2, O3) "s_mov_b64 exec, s[58:59]" \
    : P_OUT7(v), "=&{v[156:159]}"(v[7]), "=&{v[160:163]}"(u[0]), "=&{v[164:167]}"(u[1]), "=&{v[168:171]}"(u[2]), "=&{v[172:175]}"(u[3]), [t] "=&v"(t_) \
    : P_IN(vo_, sb_, tag_), [sc] "s"(sb_ + 16384), [vq] "v"(vq_), [sq] "s"(sq_) : Q_CLOB); }
#define POLL3U8(u, vq_, sq_, U0, U1, U2, v, vo_, sb_, tag_, O0, O1, O2, O3) { u32 t_; asm volatile(P_HEAD P_INI8 P_INI(160, 0) P_INI(160, 1) P_INI(160, 2) \
    ".Lpt_%=:\n s_waitcnt vmcnt(" STR(PT2) ")\n" Q_CHK(0) Q_CHK(1) Q_CHK(2) P_CHK8 \
    Q_LD(0, U0) Q_LD(1, U1) Q_LD(2, U2) P_LD8(O0, O1, O2, O3) Q_OR01 Q_OR(2) Q_TAIL \
    : P_OUT7(v), "=&{v[156:159]}"(v[7]), "=&{v[160:163]}"(u[0]), "=&{v[164:167]}"(u[1]), "=&{v[168:171]}"(u[2]), [t] "=&v"(t_) \
    : P_IN(vo_, sb_, tag_), [sc] "s"(sb_ + 16384), [vq] "v"(vq_), [sq] "s"(sq_) : Q_CLOB); }
#define PT2 0
#define POLL8C(v, vo_, sb_, tag_, O0, O1, O2, O3) { u32 t_; asm volatile(P_HEAD P_TOP P_CHK8 P_OR01 P_OR(2) P_OR(3) P_OR(4) P_OR(5) P_OR(6) P_OR(7) P_MID P_LD8(O0, O1, O2, O3) P_TAIL \
    : "+{v[128:131]}"(v[0]), "+{v[132:135]}"(v[1]), "+{v[136:139]}"(v[2]), "+{v[140:143]}"(v[3]), "+{v[144:147]}"(v[4]), "+{v[148:151]}"(v[5]), "+{v[152:155]}"(v[6]), "+{v[156:159]}"(v[7]), [t] "=&v"(t_) \
    : P_IN(vo_, sb_, tag_), [sc] "s"(sb_ + 16384) : P_CLOB); }
#define KEEP3(v) "{v[160:163]}"(v[0]), "{v[164:167]}"(v[1]), "{v[168:171]}"(v[2])

typedef u32 v32u __attribute__((ext_vector_type(32)));
typedef u32 v16u __attribute__((ext_vector_type(16)));
typedef u32 v8u __attribute__((ext_vector_type(8)));
__device__ __forceinline__ v32u perm8w(const v4u* r) {
    v32u o;
#pragma unroll
    for (int p = 0; p < 4; ++p)
#pragma unroll
        for (int j = 0; j < 8; ++j) o[4 * j + p] = __builtin_amdgcn_perm(r[2 * p + 1][j >> 1], r[2 * p][j >> 1], (j & 1) ? 0x07060302u : 0x05040100u);
    return o;
}
constexpr u32 L_G = 16384, L_U = 28672, L_DU = 49152, L_UV = 12288, L_FL = 13312;
#define FULLM " row_mask:0xf bank_mask:0xf\n"
#define F_L0(B, i, S, OFF) "global_load_dwordx4 " PV4(B, i) ", %[vo], %[" #S "] offset:" #OFF " sc1\n"
#define F_A0(B) "v_add3_u32 %[t], " PV1(B, 0, 1) ", " PV1(B, 0, 3) ", %[nt]\n"
#define F_A(B, i) "v_add3_u32 %[t], " PV1(B, i, 1) ", " PV1(B, i, 3) ", %[t]\n"
#define F_HEAD "s_nop 4\n s_mov_b32 s60, 0x10000\n"
#define F_SLOW "s_branch .Lpt_%=\n.Lsl_%=:\n s_sub_u32 s60, s60, 1\n s_cbranch_scc1 .Lgo_%=\n"
#define F_TOP "s_mov_b64 exec, -1\n.Lpt_%=:\n s_waitcnt vmcnt(0)\n"
#define F_CMP "v_cmp_ne_u32 vcc, 0, %[t]\n"
#define F_BR "s_cbranch_vccnz .Lsl_%=\n.Lgo_%=:\n"
#define F_IN(vo_, sb_, tag_, n_) [vo] "v"(vo_), [sb] "s"(sb_), [tag] "s"(tag_), [nt] "s"(0u - (n_) * (tag_))
#define F_CLOB "vcc", P_CLOB
#define MFD(j) "v[180+4*" #j ":183+4*" #j "]"
#define MF1(j) "v_mfma_f32_16x16x32_bf16 " MFD(j) ", v[212:215], v[64+4*" #j ":67+4*" #j "], 0\n"
#define MF2(j) "v_mfma_f32_16x16x32_bf16 " MFD(j) ", v[216:219], v[96+4*" #j ":99+4*" #j "], " MFD(j) "\n"
#define QBC(D, S) "v_mov_b32_dpp v[" #D "+0], " S " quad_perm:[0,0,0,0]" FULLM "v_mov_b32_dpp v[" #D "+1], " S " quad_perm:[1,1,1,1]" FULLM \
    "v_mov_b32_dpp v[" #D "+2], " S " quad_perm:[2,2,2,2]" FULLM "v_mov_b32_dpp v[" #D "+3], " S " quad_perm:[3,3,3,3]" FULLM
#define QB1(D, i, S) "v_mov_b32_dpp v[" #D "+" #i "], " S " quad_perm:[" #i "," #i "," #i "," #i "]" FULLM
#define GW(A, j, k) "ds_write2_b32 %[" #A "], v[180+4*" #j "], v[180+4*" #k "] offset0:72*(" #j "&3) offset1:72*(" #k "&3)\n"
#define GEMV2 MF2(0) "s_nop 2\n" MF2(1) "s_nop 2\n" MF2(2) "s_nop 2\n" MF2(3) GW(aw, 0, 1) "s_nop 1\n" MF2(4) "s_nop 2\n" MF2(5) GW(aw, 2, 3) "s_nop 1\n" \
    MF2(6) "s_nop 2\n" MF2(7) GW(ax, 4, 5) "s_nop 6\n" GW(ax, 6, 7) "s_waitcnt lgkmcnt(0)\n s_barrier"
#define G_IO(W0, W1, aw_) "{v[64:95]}"(W0), "{v[96:127]}"(W1), [aw] "v"(aw_), [ax] "v"((aw_) + 1152u)
#define G_TMP(d, e) "=&{v[180:211]}"(d), "=&{v[212:227]}"(e)
#define RED8 "ds_read2_b32 v[180:181], %[r0] offset0:0 offset1:72\n ds_read2_b32 v[182:183], %[r0] offset0:24 offset1:96\n" \
    "ds_read2_b32 v[184:185], %[r1] offset0:0 offset1:72\n ds_read2_b32 v[186:187], %[r1] offset0:24 offset1:96\n" \
    "ds_read2_b32 v[188:189], %[r2] offset0:0 offset1:72\n ds_read2_b32 v[190:191], %[r2] offset0:24 offset1:96\n" \
    "ds_read2_b32 v[192:193], %[r3] offset0:0 offset1:72\n ds_read2_b32 v[194:195], %[r3] offset0:24 offset1:96\n" \
    "s_waitcnt lgkmcnt(6)\n v_pk_add_f32 v[180:181], v[180:181], v[182:183]\n s_waitcnt lgkmcnt(4)\n v_pk_add_f32 v[184:185], v[184:185], v[186:187]\n" \
    "s_waitcnt lgkmcnt(2)\n v_pk_add_f32 v[188:189], v[188:189], v[190:191]\n v_pk_add_f32 v[180:181], v[180:181], v[184:185]\n" \
    "s_waitcnt lgkmcnt(0)\n v_pk_add_f32 v[192:193], v[192:193], v[194:195]\n s_nop 0\n v_pk_add_f32 v[188:189], v[188:189], v[192:193]\n s_nop 0\n"
#define PUB4 "global_store_dwordx4 %[vp], v[196:199], %[p0] sc1\n global_store_dwordx4 %[vp], v[196:199], %[p1] sc1\n" \
    "global_store_dwordx4 %[vp], v[196:199], %[p2] sc1\n global_store_dwordx4 %[vp], v[196:199], %[p3] sc1\n s_nop 1"
#define PUB_IN(p_, RS) [vp] "v"((u32)(l >> 5) * 8192u + (l & 31) * 16), [p0] "s"(p_), [p1] "s"((p_) + (RS)), [p2] "s"((p_) + 2 * (RS)), [p3] "s"((p_) + 3 * (RS))
#define RED_IN(r_) [r0] "v"(r_), [r1] "v"((r_) + 2304u), [r2] "v"((r_) + 4608u), [r3] "v"((r_) + 6912u)

#define DRY_DBG
#define DRY(LBL, PRE, ...) { v32u d0_, d1_, d2_; asm volatile(PRE "s_mov_b64 s[58:59], 0\n s_mov_b64 s[60:61], 0\n s_mov_b32 s67, 1\n s_mov_b64 exec, 0\n" \
    "s_getpc_b64 s[68:69]\n.Ldr_%=:\n s_add_u32 s68, s68, .Ldb_%=-.Ldr_%=\n s_addc_u32 s69, s69, 0\n s_branch " #LBL "\n.Ldb_%=:\n s_mov_b64 exec, -1\n s_waitcnt vmcnt(0) lgkmcnt(0)\n s_nop 7\n s_nop 7" \
    : : __VA_ARGS__ : DRY_DBG "s48", "s49", "s50", "s51", "s52", "s53", "s56", "s57", "s58", "s59", "s60", "s61", "s62", "s63", "s64", "s67", "s68", "s69", "vcc", "scc", "memory"); \
    asm volatile("" : "={v[128:159]}"(d0_), "={v[180:211]}"(d1_), "={v[212:243]}"(d2_)); }
#define KEND "; keep"
#define TS(i)
#define TSDUMP()

__global__ void __launch_bounds__(320) fused_kernel(char* __restrict__ ws, const u16* __restrict__ wmod, const u16* __restrict__ cin,
        const u16* __restrict__ wq, const u16* __restrict__ xin, const u16* __restrict__ kc, const u16* __restrict__ vc, KP8,
        const u16* __restrict__ wo, const u16* __restrict__ wg, const u16* __restrict__ wu, const u16* __restrict__ wd KPAR) {
#define KA_ONE(C) C "{s[72:73]}"(ws), C "{s[74:75]}"(wmod), C "{s[76:77]}"(cin), C "{s[78:79]}"(wq), C "{s[80:81]}"(xin), C "{s[82:83]}"(kc), C "{s[84:85]}"(vc), KA_8(C)
#define KA_TWO(C) C "{s[88:89]}"(wo), C "{s[90:91]}"(wg), C "{s[92:93]}"(wu), C "{s[94:95]}"(wd) KA_X(C)
    const auto kap = __builtin_amdgcn_kernarg_segment_ptr();
    asm volatile("s_load_dwordx16 s[72:87], %[ka], 0x0" : KA_ONE("=") : [ka] "s"(kap));
    __shared__ __attribute__((aligned(16))) char lds_[L_SIZE];
    const uint64_t t64 = __builtin_amdgcn_s_memrealtime();
    __builtin_amdgcn_sched_barrier(0);
    LAS char* const lds = (LAS char*)lds_;
    const int b = blockIdx.x, tid = threadIdx.x, l = tid & 63;
    const int w = __builtin_amdgcn_readfirstlane(tid >> 6);
    const int xcd = b & 7, idx = b >> 3;
    const u32 t0 = (u32)t64;
#define MARK(n) asm volatile(".Lfk_" #n ":")
    const int id = HMAP == 0 ? xcd * 4 + idx : HMAP == 1 ? ((xcd & 3) * 4 + idx) * 2 + (xcd >> 2) : (idx * 4 + (xcd & 3)) * 2 + (xcd >> 2);
    const int cr = RMAP == 0 ? xcd : RMAP == 1 ? (xcd + 2 * idx) & 7 : (xcd + idx) & 7, gq = (xcd + GROT) & 7;
    const bool isH = idx < 4, isR = (idx & 28) == 4, isQ = (idx & 24) == 8;
    const int slot = sel(isH, (id >> 4) * 2 + (id & 1), 4 + ((idx + 1) & 3));
    const u32 dump = (u32)(uintptr_t)lds + L_PF;
    u32 pol = POL * 8192u;
#define CH(x, STR) (((x) & 3) * (STR) + ((((u32)(x) >> 2) << 13) ^ pol))
#define CQ(x) ((x) * 16384 + ((((u32)(x) >> 2) << 13) ^ pol))
    const u32 voh = (u32)(l >> 5) * 1024u + (l & 31) * 16;
    v4u r[16]; u32 sm0, sm1; v4u xa;
#define KA_ADD(D, S, O) "s_add_u32 %[" D "], " S ", %[" O "]\n s_addc_u32 %H[" D "], " S "+1, 0\n"
    u32 role = (idx >> 4) + (w >> 2) * 2 - 0u;
    role = role == 3 ? 2u : role == 2 ? 3u : role;
    asm volatile("" : "+s"(role));
    if (role == 2) {
        if ((idx & (PFC - 1)) == 0) {
        const char* pc; u32 len;
        asm volatile("s_getpc_b64 %0\n.Lfk_0:\n s_mov_b32 %1, .Lfk_1-.Lfk_0\n s_max_i32 %1, %1, .Lfk_2-.Lfk_0\n s_max_i32 %1, %1, .Lfk_3-.Lfk_0\n"
                     " s_max_i32 %1, %1, .Lfk_4-.Lfk_0\n s_max_i32 %1, %1, .Lfk_5-.Lfk_0\n s_max_i32 %1, %1, .Lfk_6-.Lfk_0" : "=s"(pc), "=s"(len) : : "scc");
        asm volatile("s_mov_b32 m0, %4\n s_nop 0\n global_load_lds_dword %0, %3\n global_load_lds_dword %1, %3\n global_load_lds_dword %2, %3"
                     : : "v"(min((u32)l * 128u, len)), "v"(min((u32)l * 128u + 8192u, len)), "v"(min((u32)l * 128u + 16384u, len)), "s"(pc), "s"((u32)(uintptr_t)lds + L_PF) : "memory");
    }
        u32 po = (u32)(l >> 3) * 8192u + (l & 7) * 128, p1 = po + 65536u, fs = 32 * (idx - 16) * 8192 + gq * 1024;
        asm volatile("" : "+v"(po), "+v"(p1));
        u32 wl, wh; uint64_t tq;
        asm volatile("s_waitcnt lgkmcnt(0)\n s_memrealtime %[t]\n s_add_u32 %[wl], s78, %[fs]\n s_addc_u32 %[wh], s79, 0\n s_waitcnt lgkmcnt(0)"
                     : [wl] "=&s"(wl), [wh] "=&s"(wh), [t] "=&s"(tq) : [fs] "s"(fs), KA_ONE("") : "scc");
        const char* wb = (const char*)(((uint64_t)wh << 32) | wl);
        TS(12);
        if (HSP && HSP < 6) {
            const u32 k = slow_line(wmod);
            if (HSP >= 3) pf1((const char*)wmod + ((idx - 16) * 32 * 6144 + xcd * 256), (u32)(l >> 1) * 6144u + (l & 1) * 128u, dump);
            else if ((u32)(xcd & 3) == k >> 1) pf1((const char*)wmod + ((idx - 16) * 32 * 6144 + (8 * (xcd >> 2) + k) * 128), (u32)(l & 31) * 6144u, dump);
            if (HSP != 1 && HSP != 5 && (HSP == 4 || (u32)xcd == k)) { pf2((const char*)wmod + ((idx - 16) * 32 * 6144 + 2048 + xcd * 128), (u32)(l & 31) * 6144u + (l >> 5) * 1024u, (u32)(l & 31) * 6144u + (l >> 5) * 1024u + 2048u, dump); }
        }
        if (TW & 1) pf1((const char*)vc + (u32)((AGRP(xcd, idx - 16) * 65536 + 61440 + (idx - 16) * 256) * 256), 0u, dump);
        wait_until((u32)tq, T_PQ);
        pf2(wb, po, p1, dump);
        pf2(wb + 16 * 8192, po, p1, dump);
    }
    asm volatile("" : "+s"(role));
    if (role == 0) {
        const u32 st = sel(isQ, 8192, 6144);
        const u32 row0 = sel(isH, (id & 1) * 256 + 64 * w, 128 * w);
        u32 fs = row0 * st + sel(isQ, gq * 1024 + (idx - 8) * 128, sel(isH, (id >> 1) * 128, (slot - 2) * 1024 + cr * 128));
        u32 f2 = sel(isQ, 16 * 8192, 64 * 6144), nQ = (idx >> 3) ^ 1, nH = idx >> 2, fo[8];
        fo[0] = (u32)sel(isQ, 4 * (l >> 4) + ((l >> 3) & 1), l >> 3) * (8 * st) + (l & 7) * 16;
#pragma unroll
        for (int i = 1; i < 8; ++i) fo[i] = fo[i - 1] + st;
        u32 fx = 2 * sel(isQ, 128 * w + 2 * l, row0 + 8 * (l >> 3) + 2 * (l & 3)), fy = (fx + 128) & 1020u;
        asm volatile("" : "+v"(fo[0]), "+v"(fo[1]), "+v"(fo[2]), "+v"(fo[3]), "+v"(fo[4]), "+v"(fo[5]), "+v"(fo[6]), "+v"(fo[7]), "+v"(fx), "+v"(fy));
        asm volatile("" : "+s"(f2), "+s"(nH));
        u32 wl, wh, sl, sh; uint64_t tq;
        asm volatile("s_cmp_eq_u32 %[nq], 0\n s_waitcnt lgkmcnt(0)\n s_memrealtime %[t]\n s_cselect_b32 %[sl], s80, s76\n s_cselect_b32 %[sh], s81, s77\n"
                     "s_cselect_b32 %[wl], s78, s74\n s_cselect_b32 %[wh], s79, s75\n s_add_u32 %[wl], %[wl], %[fs]\n s_addc_u32 %[wh], %[wh], 0"
                     : [wl] "=&s"(wl), [wh] "=&s"(wh), [sl] "=&s"(sl), [sh] "=&s"(sh), [t] "=&s"(tq) : [nq] "s"(nQ), [fs] "s"(fs), KA_ONE("") : "scc");
        const char* wb = (const char*)(((uint64_t)wh << 32) | wl);
        const char* sp = (const char*)(((uint64_t)sh << 32) | sl);
        TS(12);
        if (nH) {
            if (HSP >= 6 && !nQ) {
                pf1((const char*)wmod + (((idx - 8) * 64 + w * 16) * 6144 + xcd * 256), (u32)(l >> 1 & 15) * 6144u + (l & 1) * 128u, dump);
                if (HSP == 8 || (HSP == 7 && (u32)xcd == slow_line(wmod))) pf1((const char*)wmod + (((idx - 8) * 64 + w * 16) * 6144 + 2048 + xcd * 128), (u32)(l & 15) * 6144u + (l >> 4) * 1024u, dump);
            }
            asm volatile("s_waitcnt lgkmcnt(0)" : "+s"(tq));
            wait_until((u32)tq, nQ ? T_R : T_Q);
        }
        TS(13);
        sm0 = *(const GAS u32*)(sp + fx);
        if (XE && nH) xa = *(const GAS v4u*)(sp + (u32)l * 16);
        __builtin_amdgcn_sched_barrier(0);
#pragma unroll
        for (int i = 0; i < 8; ++i) r[i] = ldnt(wb, fo[i]);
        if (nH) {
            asm volatile("" : "+v"(fo[0]), "+v"(fo[1]), "+v"(fo[2]), "+v"(fo[3]), "+v"(fo[4]), "+v"(fo[5]), "+v"(fo[6]), "+v"(fo[7]));
#pragma unroll
            for (int i = 0; i < 8; ++i) r[8 + i] = ldnt(wb + f2, fo[i]);
            if (!XE) xa = *(const GAS v4u*)(sp + (u32)l * 16);
            sm1 = *(const GAS u32*)(sp + fy);
        }
        __builtin_amdgcn_sched_barrier(0);
        asm volatile("s_waitcnt lgkmcnt(0)" : : "s"(tq));
        TS(14);
    }
    asm volatile("" : "+s"(role));
    if (role == 1) {
        __builtin_amdgcn_s_waitcnt(0x0F70);
        const int s = idx - 16, km = KM0;
        u32 co[4];
#pragma unroll
        for (int T = 0; T < 4; ++T) {
            const int g = AGRP(xcd, s);
            const int rot = (km & 3) * (s & 1) + (km >> 2 & 3) * (s >> 1 & 1) + (km >> 4 & 3) * (s >> 2 & 1) + (km >> 6 & 3) * (s >> 3);
            const int Tr = (T + rot) & 3;
            co[T] = NEAR ? (g * 65536 + 61440) * 256 + ((s & 7) * 16 + 4 * w + 2 * (Tr >> 1)) * 8192 + (Tr & 1) * 4096
                         : (g * 65536 + 61440) * 256 + (CMAP == 0 ? (4 * s + w) * 4 + Tr : CMAP == 5 ? (16 * w + s) * 4 + Tr : CMAP == 6 ? (4 * Tr + w) * 16 + (s ^ (Tr & 1) * 2) : CMAP == 7 ? (4 * w + Tr) * 16 + (s ^ (Tr & 1) * 2) : CMAP == 8 ? (16 * ((w + s) & 3) + s) * 4 + Tr : CMAP == 1 ? (4 * Tr + w) * 16 + s : CMAP == 2 ? (4 * w + Tr) * 16 + s : CMAP == 3 ? (4 * Tr + ((w + (s >> 2)) & 3)) * 16 + s : ((4 * (Tr >> 1) + w) * 16 + s) * 2 + (Tr & 1)) * 4096;
        }
        u32 kw = (u32)(uintptr_t)lds + w * 16384, vl = (u32)l * 16;
        u32 k0o = (u32)(l >> 4) * 256 + (((l & 15) ^ (4 * (l >> 4))) * 16), k1o = k0o ^ 16u, k2o = k0o ^ 32u, k3o = k0o ^ 48u;
        asm volatile("" : "+v"(k0o), "+v"(k1o), "+v"(k2o), "+v"(k3o), "+v"(vl));
        asm volatile("" : "+s"(co[0]), "+s"(co[1]), "+s"(co[2]), "+s"(co[3]), "+s"(kw));
        const char *kp, *vp; uint64_t tq;
        char* wsa; const char* wda; u32 tag;
        asm volatile("s_waitcnt lgkmcnt(0)\n s_memrealtime %[t]\n s_mov_b64 %[k], s[82:83]\n s_mov_b64 %[v], s[84:85]\n s_mov_b64 %[w], s[72:73]\n s_waitcnt lgkmcnt(0)"
                     : [k] "=&s"(kp), [v] "=&s"(vp), [w] "=&s"(wsa), [t] "=&s"(tq) : KA_ONE(""));
        if (NEAR) { const u32 par = (((u32)xcd >> 2) ^ OPK ^ (OOM & 1)) << 13; kp += par; vp += par; }
        TS(13);
        wait_until((u32)tq, T_K);
        TS(14);
#pragma unroll
        for (int T = 0; T < 4; ++T) {
            if (T == 1) wait_until((u32)tq, T_K2);
            asm volatile("s_mov_b32 m0, %5\n s_waitcnt vmcnt(%6)\n"
                         "global_load_lds_dwordx4 %0, %4 nt\n global_load_lds_dwordx4 %1, %4 offset:1024 nt\n s_waitcnt vmcnt(%6)\n"
                         "global_load_lds_dwordx4 %2, %4 offset:2048 nt\n global_load_lds_dwordx4 %3, %4 offset:3072 nt"
                         : : "v"(k0o), "v"(k1o), "v"(k2o), "v"(k3o), "s"(kp + co[T]), "s"(kw + T * 4096), "n"(WIN) : "memory");
        }
        asm volatile("s_load_dword %0, %2, 0x0\n s_load_dwordx2 %1, %3, 0x58" : "=&{s51}"(tag), "=&{s[52:53]}"(wda) : "s"(wsa + OFF_EP), "s"(kap) : "memory");
        const int g = AGRP(xcd, s), xq = (g - GROT) & 7;
        const u32 lb = (u32)(uintptr_t)lds;
        const u32 ka = kw + (l & 3) * 1024 + ((l >> 2) & 3) * 256 + (((((l >> 2) & 3) << 2) | ((l >> 4) ^ (l & 3))) * 16);
        asm volatile("s_mov_b64 s[58:59], 0xffff\n s_mov_b64 s[60:61], -1\n s_mov_b32 s67, 0\n s_branch .Ls3"
            : "=v"(r[0]), "=v"(r[1]), "=v"(r[2]), "=v"(r[3]), "=v"(r[4]), "=v"(r[5]), "=v"(r[6]), "=v"(r[7]), "=v"(r[8]), "=v"(r[9]), "=v"(r[10]), "=v"(r[11]),
              "=v"(r[12]), "=v"(r[13]), "=v"(r[14]), "=v"(r[15]), "=v"(sm0), "=v"(sm1), "=v"(xa)
            : "{v240}"(lb + L_Q + (l & 3) * 320 + (l >> 4) * 16), "{v241}"(l < 4 ? lb + L_ST + l * 32 + w * 8 : lb + L_PF + l * 8), "{v242}"(kw + l * 16), "{v243}"(lb + tid * 16),
              "{v244}"(lb + L_F), "{v245}"((u32)(tid >> 5) * 32768 + (tid & 31) * 16 + ((xq >> 2) << 13)), "{v247}"(vl), "{v248}"(ka), "{v252}"(min((u32)l * 128u, 7u * 1024 - 128)),
              "{s[40:41]}"(vp + co[0]), "{s[42:43]}"(vp + co[1]), "{s[44:45]}"(vp + co[2]), "{s[46:47]}"(vp + co[3]), "{s50}"(t0), "{s51}"(tag), "{s[52:53]}"(wda),
              "{s[54:55]}"(wsa + OFF_XA + (xq & 3) * 131072 + (s >> 3) * 16384 + (s & 7) * 1024), "{s64}"(w), "{s65}"((224 * xcd + 14 * s + 7 * (w & 1)) * 1024), "{s66}"(dump)
            : "s58", "s59", "s60", "s61", "s67", "memory");
    }
    asm volatile("" :: "s"(t64));
    asm volatile("s_waitcnt lgkmcnt(0)\n s_load_dwordx8 s[88:95], %[ka], 0x40" KA_L : KA_ONE("+"), KA_TWO("=") : [ka] "s"(kap));
    u32 eps;
    asm volatile("s_load_dword %0, %1, 0x0" : "=s"(eps) : "s"(ws + OFF_EP) : "memory");
    auto epc = [&]() __attribute__((always_inline)) { u32 e; asm volatile("s_waitcnt lgkmcnt(0)\n s_mov_b32 %0, %1" : "=s"(e) : "s"(eps) : "memory"); return e; };
#define epoch epc()
#define NEXT(e) ((e) * 1664525u + 1013904223u)
#define TAG (NEXT((u32)__builtin_amdgcn_readfirstlane(epoch)) & ~0x40004000u)
#define TAGC (NEXT(epc()) & ~0x40004000u)

    auto getpol = [&]() __attribute__((always_inline)) {
        const u32 tag = TAGC;
        v4u u;
        _Pragma("unroll 1") for (int spin = 0; spin < SPIN_MAX; ++spin) {
            ld16<0>(u, ws + OFF_PL + xcd * 1024, 0u);
            wvm<0>(u);
            if (__builtin_amdgcn_readfirstlane(badtag(u, tag)) == 0) break;
            __builtin_amdgcn_s_sleep(4);
        }
        pol = __builtin_amdgcn_readfirstlane(u.x) > __builtin_amdgcn_readfirstlane(u.z) ? 0u : 8192u;
        if (l == 0) *(LAS u32*)(lds + L_PL) = pol;
        TS(15);
    };
#define LDPOL() pol = __builtin_amdgcn_readfirstlane(*(const volatile LAS u32*)(lds + L_PL))
    u32 dloc = 0;
    {
        u32 tv, sv; uint64_t ta, tb; v2u pv;
        asm volatile("s_cmp_eq_u32 %[who], 8 * 8 + 4\n s_cbranch_scc0 .Lcs_%=\n s_mov_b64 exec, 1\n v_mov_b32 %[t], 0x2000\n"
                     "global_store_dword %[lo], %[t], %[cb] sc1\n global_store_dword %[t], %[t], %[cb] sc1\n s_mov_b64 exec, 0xff\n"
                     "s_waitcnt vmcnt(1)\n s_memtime %[ta]\n s_waitcnt vmcnt(0)\n s_memtime %[tb]\n s_waitcnt lgkmcnt(0)\n"
                     "s_sub_u32 %[d], s62, s60\n s_cmp_eq_u32 %[pub], " STR(CALB) "\n s_cbranch_scc0 .Lct_%=\n"
                     "s_mul_i32 %[s], %[ep], 1664525\n s_add_u32 %[s], %[s], 1013904223\n s_and_b32 %[s], %[s], 0xbfffbfff\n v_mov_b32 v129, %[s]\n"
                     "v_mov_b32 v128, %[d]\n global_store_dwordx2 %[lo], v[128:129], %[pl] sc1\n.Lct_%=:\n s_mov_b64 exec, -1\n.Lcs_%=:"
                     : [t] "=&v"(tv), [s] "=&s"(sv), [ta] "=&{s[60:61]}"(ta), [tb] "=&{s[62:63]}"(tb), "=&{v[128:129]}"(pv), [d] "+s"(dloc)
                     : [who] "s"((idx & 24) * 8 + w), [pub] "s"(b & ~4), [lo] "v"((u32)l * 1024u), [cb] "s"(ws + OFF_CAL + (b & 63) * 128), [pl] "s"(ws + OFF_PL + (xcd >> 2) * 8), [ep] "s"(eps) : "scc", "memory");
        if ((b & ~4) == CALB) TS(11);
    }
    asm volatile("s_waitcnt lgkmcnt(0)" : KA_TWO("+"));
    if ((TW & 2) && isQ && w == 4) { wait_until(t0, T_TW); pf1((const char*)wo + (u32)(gq * 524288 + (idx - 8) * 128), (u32)(l & 7) * 65536u, dump); }
    auto pace = [&]() __attribute__((always_inline)) {
        const u32 tag = TAGC;
        if ((TW & 4) && isH) {
            pf1((const char*)wg + (xcd * 7 + idx) * 64, (u32)(l & 31) * 57344u, dump);
            pf1((const char*)wu + (xcd * 7 + idx) * 64, (u32)(l & 31) * 57344u, dump);
            pf1((const char*)wd + xcd * 229376, (u32)(l & 3) * 65536u, dump);
        }
        wait_until(t0, T_MP);
        const u32 vo = l < 32 ? OFF_XQ + gq * 16384 + (idx >> 1) * 1024 + (idx & 1) * 256 : OFF_PL + xcd * 1024;
        v4u u;
        _Pragma("unroll 1") for (int spin = 0; spin < SPIN_MAX; ++spin) {
            ld16<0>(u, ws, vo);
            wvm<0>(u);
            if (__all(badtag(u, tag) == 0)) break;
            __builtin_amdgcn_s_sleep(8);
        }
        pol = __builtin_amdgcn_readlane(u.x, 32) > __builtin_amdgcn_readlane(u.z, 32) ? 0u : 8192u;
        *(LAS u32*)(lds + L_PL) = pol;
        TS(4);
        wait_until(now(), D_PW);
        pf1((const char*)wo + ((512 * gq + 64 * idx) * 1024 + slow_line(wo) * 128), (u32)l * 1024u, dump);
        auto slm = [&]() __attribute__((always_inline)) {
            const char *pg = (const char*)wg + ((7 * xcd) >> 1) * 128, *pu = (const char*)wu + ((7 * xcd) >> 1) * 128;
            const u32 kg = slow_line(pg), ku = slow_line(pu);
            pf1(pg + ((64 * idx + (kg >> 2)) * 3584 + (kg & 3) * 128), (u32)(l & 31) * 7168u, dump);
            pf1(pu + ((64 * idx + (ku >> 2)) * 3584 + (ku & 3) * 128), (u32)(l & 31) * 7168u, dump);
        };
        if (SLM == 1) slm();
        const char* so = (const char*)wo + (size_t)gq * 524288 + 262144 + idx * 32768;
        pf2(so, (u32)l * 128u, (u32)l * 128u + 8192u, dump);
        pf2(so + 16384, (u32)l * 128u, (u32)l * 128u + 8192u, dump);
        if (SLM == 2) slm();
    };
    if (idx < 8 && w < 4) {
        TS(1);
        v4f acc = mod_compute(v4f{0.f, 0.f, 0.f, 0.f}, r, sm0, l);
        if (isR) acc = mod_compute(acc, r + 8, sm1, l);
        if (((l >> 3) & 1) == (l >> 5)) *(LAS v4f*)(lds + L_R0 + (2 * w + (l >> 5)) * 256 + (l & 7) * 32 + ((l >> 4) & 1) * 16) = acc;
        TS(2);
        bar();
    }
    if (idx < 8 && w == 4) {
        const u32 tag = TAGC;
        const int cb = sel(isH, (id >> 1) & 7, cr);
        TS(1);
        bar();
        TS(3);
        pub_f32<65536>(ws + (sel(isH, OFF_XH, OFF_XR) + (cb >> 1) * 16384 + (slot & 3) * 2048 + (cb & 1) * 1024), redcols<8>(lds + L_R0, l), tag, l);
        TS(2);
        MARK(2);
    }
    if (idx < 8 && w == 4) { *(LAS u32*)(lds + L_FL + (l & 3) * 4) = 0u; pace(); }
    if (idx < 7 && (w == 4 || !ME)) bar();
    if (idx < 7) {
        const int jm = xcd * 7 + idx;
        if (w < 4) {
            v4u r0[16], pm[3];
            v32u r2;
            __builtin_amdgcn_s_waitcnt(0x0F70);
            const char* p = (const char*)((l & 4) ? wu : wg) + ((size_t)(128 * w + 1) * 3584 + jm * 64) + ((u32)(4 * (l >> 4) + ((l >> 3) & 1)) * 28672u + (l & 3) * 16);
            auto ldgu = [&](int h) __attribute__((always_inline)) {
                const char *a = p + h * 57344, *c = a + 10752, *d = a + 21504;
                ldn64<-3584>(r0[8 * h], a); ldn64<0>(r0[8 * h + 1], a); ldn64<3584>(r0[8 * h + 2], a);
                ldn64<-3584>(r0[8 * h + 3], c); ldn64<0>(r0[8 * h + 4], c); ldn64<3584>(r0[8 * h + 5], c);
                ldn64<-3584>(r0[8 * h + 6], d); ldn64<0>(r0[8 * h + 7], d);
            };
            if (ME) {
                wait_until(t0, T_ME);
                ldgu(0);
                if (ME > 8) ldgu(1);
                bar();
            }
            LDPOL();
            wait_until(now(), D_M);
            const char* sbr = ws + OFF_XR + CH(xcd, 65536) + w * 16384 + 2048;
            asm volatile("s_nop 4" : "+s"(sbr));
            ld16<-2048>(pm[0], sbr, voh); ld16<0>(pm[1], sbr, voh); ld16<2048>(pm[2], sbr, voh);
            u32 xo;
            asm volatile("global_load_dword %0, %1, %2" : "=v"(xo) : "v"((u32)l * 4u), "s"(xin + 128 * w) : "memory");
            {
                if (ME < 8) ldgu(0);
                if (ME < 16) ldgu(1);
#define D_L(i, OFF) "global_load_dwordx4 v[4*" #i ":4*" #i "+3], %[vo], %[sb] offset:" #OFF " nt\n"
                asm volatile(D_L(0, -4096) D_L(1, -3072) D_L(2, -2048) D_L(3, -1024) D_L(4, 0) D_L(5, 1024) D_L(6, 2048) D_L(7, 3072)
                             : "=&{v[0:31]}"(r2) : [vo] "v"((u32)(l >> 4) * 8192u + (l & 15) * 16), [sb] "s"((const char*)wd + (size_t)(32 * jm) * 1024 + 256 * w + 4096) : "memory");
            }
            TS(9);
            const u32 tag = TAG;
            const u32 lb = (u32)(uintptr_t)lds, ro = 72u * (l & 7) + 4u * ((l >> 3) & 1) + (l >> 4);
            u32 aw = lb + L_G + w * 2304 + l * 4, ax = aw + 1152u, au = lb + L_U + w * 4608 + l * 8, av = au + 2304u, ru = lb + L_UV + l * 8, af = lb + L_FL + w * 4;
            u32 as = (l & 31) ? lb + L_DU + l * 4 : lb + L_LN + w * 8 + (l >> 5) * 4, ar = lb + L_G + ro * 4, az = lb + L_LN, vs = (u32)(l >> 3) * 32768u + (l & 7) * 16;
            char* so = ws + OFF_XD + CH(4 * (w >> 1), 0) + (w & 1) * 262144 + (jm >> 5) * 16384 + ((jm >> 2) & 7) * 1024 + (jm & 3) * 128;
            const char *sw = ws + OFF_XW + CH(xcd, 131072) + w * 32768 + 4096, *sw2 = sw + 16384;
            v2f ce = {0.f, 1e-5f}; float kg1 = -2.3022081986f * 0.044715f, kg0 = -2.3022081986f; u32 ar2 = ar + 96u, nt = 0u - 16u * tag;
            asm volatile("" : "+v"(aw), "+v"(ax), "+v"(au), "+v"(av), "+v"(ru), "+v"(af), "+v"(as), "+v"(ar), "+v"(ar2), "+v"(az), "+v"(vs), "+s"(so), "+s"(sw), "+s"(sw2), "+s"(nt), "+v"(ce), "+v"(kg1), "+v"(kg0));
            asm volatile("s_waitcnt vmcnt(%4)" : "+v"(pm[0]), "+v"(pm[1]), "+v"(pm[2]), "+v"(xo) : "n"(24 - ME) : "memory");
            _Pragma("unroll 1") for (int spin = 0; spin < SPIN_MAX; ++spin) {
                if (__all((badtag(pm[0], tag) | badtag(pm[1], tag) | badtag(pm[2], tag)) == 0)) break;
                __builtin_amdgcn_s_sleep(4);
                ld16<-2048>(pm[0], sbr, voh); ld16<0>(pm[1], sbr, voh); ld16<2048>(pm[2], sbr, voh);
                wvm<2>(pm[0]); wvm<1>(pm[1]); wvm<0>(pm[2]);
            }
            TS(4);
            const u32 gp = pk2(asf(pm[0].x), asf(pm[0].z)), sp = pk2(asf(pm[1].x), asf(pm[1].z)), cp = pk2(asf(pm[2].x), asf(pm[2].z));
            const u32 c1 = pk2(1.f + lo16(cp), 1.f + hi16(cp));
            v2f ga = {lo16(gp), hi16(gp)}, xv = {lo16(xo), hi16(xo)}, gc = {ga[0] * lo16(c1), ga[1] * hi16(c1)}, xc = {xv[0] * lo16(c1), xv[1] * hi16(c1)};
            v32u W0, W1, WD, td, pw; v16u te;
            asm volatile("s_nop 1\n" QBC(212, "%[c]") QBC(220, "%[s]") "v_mov_b32_dpp v224, %[c] row_ror:8" FULLM "v_mov_b32_dpp v225, %[s] row_ror:8" FULLM
                         "v_cndmask_b32 v212, v212, v220, %[m]\n v_cndmask_b32 v213, v213, v221, %[m]\n v_cndmask_b32 v214, v214, v222, %[m]\n v_cndmask_b32 v215, v215, v223, %[m]\n"
                         QBC(216, "v224") QBC(220, "v225")
                         "v_cndmask_b32 v216, v216, v220, %[m]\n v_cndmask_b32 v217, v217, v221, %[m]\n v_cndmask_b32 v218, v218, v222, %[m]\n v_cndmask_b32 v219, v219, v223, %[m]"
                         : "=&{v[212:227]}"(te), "+v"(ga), "+v"(xv), "+v"(gc), "+v"(xc) : [c] "v"(c1), [s] "v"(sp), [m] "s"(0xaaaaaaaaaaaaaaaaull));
            wvm8<16>(r0); W0 = perm8w(r0);
            asm volatile("s_nop 1\n" MF1(0) MF1(1) MF1(2) MF1(3) MF1(4) MF1(5) MF1(6) MF1(7) : "=&{v[180:211]}"(td), "+{v[64:95]}"(W0) : "{v[212:227]}"(te));
#define R8(r) "+v"(r[0]), "+v"(r[1]), "+v"(r[2]), "+v"(r[3]), "+v"(r[4]), "+v"(r[5]), "+v"(r[6]), "+v"(r[7])
            asm volatile("s_waitcnt vmcnt(8)\n" F_L0(128, 0, sb, -4096) F_L0(128, 1, sb, -2048) F_L0(128, 2, sb, 0) F_L0(128, 3, sb, 2048)
                         F_L0(128, 4, sc, -4096) F_L0(128, 5, sc, -2048) F_L0(128, 6, sc, 0) F_L0(128, 7, sc, 2048)
                         : "=&{v[128:159]}"(pw), R8((r0 + 8)) : [vo] "v"(voh), [sb] "s"(sw), [sc] "s"(sw2) : "memory");
            W1 = perm8w(r0 + 8);
            TS(3);
#define UW(A, j, k) "ds_write2_b64 %[" #A "], v[180+4*" #j ":181+4*" #j "], v[180+4*" #k ":181+4*" #k "] offset0:72*(" #j "&3) offset1:72*(" #k "&3)\n"
#define PD(j, p, S) "v_perm_b32 v[32+4*" #j "+" #p "], v[8*" #p "+4+(" #j ">>1)], v[8*" #p "+(" #j ">>1)], %[" #S "]\n"
#define PD2(j, k, p) PD(j, p, pl) PD(k, p, ph)
#define PD8(p) PD2(0, 1, p) PD2(2, 3, p) PD2(4, 5, p) PD2(6, 7, p)
            asm volatile("s_nop 1\n" MF2(0) "s_nop 2\n" MF2(1) "s_nop 2\n" MF2(2) "s_nop 2\n" MF2(3) UW(au, 0, 1) "s_nop 1\n" MF2(4) "s_nop 2\n" MF2(5) UW(au, 2, 3) "s_nop 1\n"
                         MF2(6) "s_nop 2\n" MF2(7) UW(av, 4, 5) "s_nop 6\n" UW(av, 6, 7) "ds_write_b32 %[af], %[af]\n"
                         "s_waitcnt vmcnt(8)\n" PD8(0) PD8(1) PD8(2) PD8(3)
                         : "=&{v[32:63]}"(WD), "+{v[96:127]}"(W1), "+{v[180:211]}"(td)
                         : "{v[212:227]}"(te), "{v[0:31]}"(r2), [au] "v"(au), [av] "v"(av), [af] "v"(af), [pl] "s"(0x05040100u), [ph] "s"(0x07060302u) : "memory");
            TS(10);
            {
                u32 t_;
                asm volatile(F_HEAD "s_waitcnt vmcnt(0)\n" F_SLOW P_CHK8 P_LD8(-4096, -2048, 0, 2048) "s_mov_b64 exec, -1\n s_waitcnt vmcnt(" STR(PT5) ")\n.Lpt_%=:\n"
                    F_A0(128) F_A(128, 1) F_A(128, 2) F_A(128, 3) F_A(128, 4) F_A(128, 5) F_A(128, 6) F_A(128, 7) F_CMP
                    "v_add_f32 v212, v128, v132\n v_add_f32 v213, v130, v134\n" F_BR
                    "v_add_f32 v214, v136, v140\n v_add_f32 v215, v138, v142\n v_add_f32 v216, v144, v148\n v_add_f32 v217, v146, v150\n"
                    "v_add_f32 v218, v152, v156\n v_add_f32 v219, v154, v158\n v_pk_add_f32 v[212:213], v[212:213], v[214:215]\n"
                    "v_pk_add_f32 v[216:217], v[216:217], v[218:219]\n s_nop 0\n v_pk_add_f32 v[212:213], v[212:213], v[216:217]\n s_nop 0\n"
                    "v_pk_fma_f32 v[214:215], %[gc], v[212:213], %[xc]\n v_pk_fma_f32 v[220:221], %[ga], v[212:213], %[x]\n"
                    "v_cvt_pk_bf16_f32 v222, v214, v215\n v_add_f32 v223, v220, v221\n v_mul_f32 v224, v220, v220\n" QBC(212, "v222")
                    "v_mov_b32_dpp v225, v222 row_ror:8" FULLM "v_fmac_f32 v224, v221, v221\n"
                    MF1(0) QB1(216, 0, "v225") QB1(216, 1, "v225") QB1(216, 2, "v225")
                    MF1(1) QB1(216, 3, "v225") "v_permlane32_swap_b32 v223, v224\n v_add_f32 v223, v223, v224\n"
                    MF1(2) "v_mov_b32 v224, v223\n s_nop 0\n"
                    MF1(3) "v_permlane16_swap_b32 v223, v224\n v_add_f32 v223, v223, v224\n s_nop 0\n"
                    MF1(4) "v_add_f32_dpp v223, v223, v223 row_ror:8" FULLM "s_nop 0\n"
                    MF1(5) "v_add_f32_dpp v223, v223, v223 row_ror:4" FULLM "s_nop 0\n"
                    MF1(6) "v_add_f32_dpp v223, v223, v223 quad_perm:[2,3,0,1]" FULLM "s_nop 0\n"
                    MF1(7) "v_add_f32_dpp v223, v223, v223 quad_perm:[1,0,3,2]" FULLM "ds_write_b32 %[as], v223\n" GEMV2
                    : G_TMP(td, te), "+{v[128:159]}"(pw), [t] "=&v"(t_)
                    : "{v[64:95]}"(W0), "{v[96:127]}"(W1), [aw] "v"(aw), [ax] "v"(ax), [vo] "v"(voh), [sb] "s"(sw), [sc] "s"(sw2), [tag] "s"(tag), [nt] "s"(nt),
                      [gc] "v"(gc), [xc] "v"(xc), [ga] "v"(ga), [x] "v"(xv), [as] "v"(as) : F_CLOB);
            }
            TS(7);
#define MFW(D, j) "v_mfma_f32_16x16x32_bf16 v[" #D ":" #D "+3], v[192:195], v[32+4*" #j ":35+4*" #j "], 0\n"
#define MRG(D, S, R) "v_mov_b32_dpp v" #D ", v" #S " quad_perm:[0,1,2,3] row_mask:" #R " bank_mask:0xf\n"
            asm volatile("ds_read_b128 v[180:183], %[az]\n ds_read_b128 v[184:187], %[az] offset:16\n ds_read_b64 v[196:197], %[ru]\n"
                "ds_read2st64_b32 v[188:189], %[ar] offset0:0 offset1:9\n ds_read2st64_b32 v[190:191], %[ar] offset0:18 offset1:27\n"
                "ds_read2st64_b32 v[192:193], %[as] offset0:0 offset1:9\n ds_read2st64_b32 v[194:195], %[as] offset0:18 offset1:27\n"
                "s_waitcnt lgkmcnt(5)\n v_pk_add_f32 v[180:181], v[180:181], v[182:183]\n v_pk_add_f32 v[184:185], v[184:185], v[186:187]\n s_nop 0\n"
                "v_pk_add_f32 v[180:181], v[180:181], v[184:185]\n s_nop 0\n v_pk_fma_f32 v[180:181], v[180:181], %[ci], %[ce] op_sel_hi:[1,0,1]\n s_nop 0\n"
                "v_fma_f32 v181, -v180, v180, v181\n v_max_f32 v181, 0x3727c5ac, v181\n v_rsq_f32 v181, v181\n"
                "s_waitcnt lgkmcnt(0)\n v_pk_add_f32 v[188:189], v[188:189], v[190:191]\n v_pk_add_f32 v[192:193], v[192:193], v[194:195]\n"
                "v_fma_f32 v190, -v180, v196, v188\n v_add_f32 v192, v192, v193\n v_add_f32 v190, v190, v189\n v_add_f32 v190, v190, v192\n"
                "v_fma_f32 v188, v190, v181, v197\n"
                "v_mul_f32 v189, v188, v188\n v_fma_f32 v189, v189, %[k1], %[k0]\n v_mul_f32 v189, v189, v188\n v_exp_f32 v189, v189\n"
                "v_mul_f32_dpp v190, v188, v188 row_ror:8" FULLM "v_add_f32 v189, 1.0, v189\n v_rcp_f32 v189, v189\n"
                "v_mov_b32_dpp v191, v190 quad_perm:[1,0,3,2]" FULLM "v_mul_f32 v190, v190, v189\n v_mul_f32_dpp v191, v189, v191 quad_perm:[1,0,3,2]" FULLM
                "v_cvt_pk_bf16_f32 v190, v190, v191\n s_nop 1\n"
                "v_mov_b32_dpp v192, v190 row_newbcast:0" FULLM "v_mov_b32_dpp v193, v190 row_newbcast:2" FULLM
                "v_mov_b32_dpp v194, v190 row_newbcast:4" FULLM "v_mov_b32_dpp v195, v190 row_newbcast:6" FULLM "s_nop 1\n"
                MFW(196, 0) "s_nop 2\n" MFW(198, 1) "s_nop 2\n" MFW(204, 2) "s_nop 2\n" MFW(208, 3) "v_mov_b32 v197, %[tag]\n v_mov_b32 v199, %[tag]\n s_nop 0\n"
                MFW(212, 4) MRG(196, 204, 0x2) "s_nop 1\n" MFW(216, 5) MRG(198, 208, 0x2) "s_nop 1\n" MFW(220, 6) MRG(196, 212, 0x4) "s_nop 1\n"
                MFW(224, 7) MRG(198, 216, 0x4) "s_nop 2\n" MRG(196, 220, 0x8) "s_nop 2\n" MRG(198, 224, 0x8)
                "global_store_dwordx4 %[vs], v[196:199], %[so] sc1\n s_nop 1"
                : G_TMP(td, te) : "{v[32:63]}"(WD), [az] "v"(az), [ar] "v"(ar), [as] "v"(ar2), [ru] "v"(ru), [ci] "s"(0x3b0000003b000000ull), [ce] "v"(ce),
                  [k1] "v"(kg1), [k0] "v"(kg0), [tag] "s"(tag), [vs] "v"(vs), [so] "s"(so) : "memory");
            TS(8);
            asm volatile(KEND ::: "memory");
            MARK(1);
        } else {
            const u32 lb = (u32)(uintptr_t)lds, ru = lb + L_U + (72u * (l & 7) + 4u * ((l >> 3) & 1) + (l >> 4)) * 8;
            v16u td;
            asm volatile(".Lfl_%=:\n s_sleep 1\n ds_read_b32 v180, %[af]\n s_waitcnt lgkmcnt(0)\n v_cmp_eq_u32 vcc, 0, v180\n s_cbranch_vccnz .Lfl_%=\n"
                         "ds_read2st64_b64 v[180:183], %[ru] offset0:0 offset1:9\n ds_read2st64_b64 v[184:187], %[ru] offset0:18 offset1:27\n"
                         "ds_read2st64_b64 v[188:191], %[rv] offset0:0 offset1:9\n ds_read2st64_b64 v[192:195], %[rv] offset0:18 offset1:27\n"
                         "s_waitcnt lgkmcnt(2)\n v_pk_add_f32 v[180:181], v[180:181], v[182:183]\n v_pk_add_f32 v[184:185], v[184:185], v[186:187]\n"
                         "s_waitcnt lgkmcnt(0)\n v_pk_add_f32 v[188:189], v[188:189], v[190:191]\n v_pk_add_f32 v[192:193], v[192:193], v[194:195]\n"
                         "v_pk_add_f32 v[180:181], v[180:181], v[184:185]\n v_pk_add_f32 v[188:189], v[188:189], v[192:193]\n s_nop 0\n v_pk_add_f32 v[180:181], v[180:181], v[188:189]\n"
                         "s_nop 0\n ds_write_b64 %[uv], v[180:181]\n s_waitcnt lgkmcnt(0)\n s_barrier"
                         : "=&{v[180:195]}"(td) : [af] "v"(lb + L_FL + (l & 3) * 4), [ru] "v"(ru), [rv] "v"(ru + 192u), [uv] "v"(lb + L_UV + l * 8) : "vcc", "memory");
            TS(5);
        }
    } else if (isQ) {
        const int c = idx - 8, g = gq;
        if (w < 4) {
            const u32 xo = sm0;
            TS(1);
            float n0, n1;
            {
                float xv[8] = {lo16(xa.x), hi16(xa.x), lo16(xa.y), hi16(xa.y), lo16(xa.z), hi16(xa.z), lo16(xa.w), hi16(xa.w)};
                float sm = ((xv[0] + xv[1]) + (xv[2] + xv[3])) + ((xv[4] + xv[5]) + (xv[6] + xv[7]));
                sm += dpp<QP1>(sm); sm += dpp<QP2>(sm); sm += dpp<ROR(4)>(sm); sm += dpp<ROR(8)>(sm); sm = x32add(x16add(sm));
                const float mean = sm * (1.f / 512.f);
                float v = 0.f;
#pragma unroll
                for (int i = 0; i < 8; ++i) { const float d = xv[i] - mean; v += d * d; }
                v += dpp<QP1>(v); v += dpp<QP2>(v); v += dpp<ROR(4)>(v); v += dpp<ROR(8)>(v); v = x32add(x16add(v));
                const float mb = rbf(mean);
                const float dn = rbf(__builtin_sqrtf(rbf(rbf(v * (1.f / 512.f)) + 1e-5f)));
                n0 = rbf(rbf(lo16(xo) - mb) / dn); n1 = rbf(rbf(hi16(xo) - mb) / dn);
            }
            v4u o0[8], o1[8];
            v4u ph[4];
            perm8(o0, r); perm8(o1, r + 8);
            pin8(o0); pin8(o1);
            asm volatile("" : : "v"(sm1));
            const u32 tag = TAG;
            TS(2);
            {
                float sa0, sa1, ca0, ca1;
                pol = __builtin_amdgcn_readfirstlane(*(const volatile LAS u32*)(lds + L_PL)) & 8192u;
                const char* sb = ws + OFF_XH + CH(xcd, 65536) + w * 16384 + 4096;
                POLL4(ph, voh, sb, tag, -4096, -2048, 0, 2048);
                sa0 = asf(ph[0].x) + asf(ph[1].x); sa1 = asf(ph[0].z) + asf(ph[1].z);
                ca0 = asf(ph[2].x) + asf(ph[3].x); ca1 = asf(ph[2].z) + asf(ph[3].z);
                TS(3);
                const u32 sp = pk2(sa0, sa1), cp = pk2(ca0, ca1);
                const u32 c1 = pk2(1.f + lo16(cp), 1.f + hi16(cp));
                const u32 m = pk2(n0 * lo16(c1), n1 * hi16(c1));
                const u32 h = pk2(lo16(m) + lo16(sp), hi16(m) + hi16(sp));
                gemv64(lds + L_R0, v4u{dppu<QB(0)>(h), dppu<QB(1)>(h), dppu<QB(2)>(h), dppu<QB(3)>(h)}, o0, o1, w, l);
            }
            bar();
            TS(4);
            bar();
            DRAIN4(ph);
            wait_until(now(), D_WO);
            v4u r0[16];
            {
                const char* sb = (const char*)wo + (size_t)(g * 512 + 2 * w) * 1024 + c * 128;
                const u32 vo = (u32)(((l >> 3) & 1) * 128 + 32 * (l >> 4)) * 1024u + (l & 7) * 16;
#pragma unroll
                for (int h = 0; h < 2; ++h) {
#pragma unroll
                    for (int i = 0; i < 4; ++i) ldn<0>(r0[8 * h + i], sb + h * 262144, vo + i * 8192);
#pragma unroll
                    for (int i = 0; i < 4; ++i) ldn<1024>(r0[8 * h + 4 + i], sb + h * 262144, vo + i * 8192);
                }
            }
            bar();
            LDPOL();
            const char *sq = ws + OFF_XS + CQ(xcd), *sa = ws + OFF_XA + CH(xcd, 131072) + w * 32768 + 4096, *sc = sa + 16384;
            u32 vq = (u32)(l >> 4) * 128 + ((l >> 2) & 3) * 16, va = (u32)(l >> 4) * 1024 + (l & 15) * 16, aw = (u32)(uintptr_t)lds + L_G + w * 2304 + l * 4, ax = aw + 1152u;
            u32 n8 = 0u - 8u * tag, n16 = 0u - 16u * tag;
            const int hd = (l >> 2) & 3;
            u32 k0 = hd == 0 ? 0x0000ffffu : hd == 1 ? 0xffff0000u : 0u, k2 = hd == 2 ? 0x0000ffffu : hd == 3 ? 0xffff0000u : 0u;
            asm volatile("s_nop 4" : "+s"(sq), "+s"(sa), "+s"(sc), "+s"(n8), "+s"(n16), "+v"(vq), "+v"(va), "+v"(aw), "+v"(ax), "+v"(k0), "+v"(k2));
            v32u W0, W1, pa, td; v16u ps, te;
#define XS_LD Q_L0(0, 0) Q_L0(1, 1024) Q_L0(2, 2048) Q_L0(3, 3072)
#define XA_LD Q_L1(0, sb, -4096) Q_L1(1, sb, -3840) Q_L1(2, sb, 0) Q_L1(3, sb, 256) Q_L1(4, sc, -4096) Q_L1(5, sc, -3840) Q_L1(6, sc, 0) Q_L1(7, sc, 256)
#define XS_CK F_A0(160) F_A(160, 1) F_A(160, 2) F_A(160, 3) F_CMP
            asm volatile("s_waitcnt vmcnt(8)\n" XS_LD : "=&{v[160:175]}"(ps), R8(r0) : [vq] "v"(vq), [sq] "s"(sq) : "memory");
            W0 = perm8w(r0);
            asm volatile("" : "+{v[64:95]}"(W0));
            wvm8<4>(r0 + 8);
            W1 = perm8w(r0 + 8);
            asm volatile("" : "+{v[96:127]}"(W1));
            TS(5);
            {
                u32 t_;
                asm volatile("s_mov_b32 s60, 0x10000\n s_waitcnt vmcnt(0)\n" F_SLOW XS_LD "s_waitcnt vmcnt(" STR(PTS) ")\n.Lpt_%=:\n" XS_CK F_BR XA_LD
                    : "+{v[160:175]}"(ps), "=&{v[128:159]}"(pa), [t] "=&v"(t_) : [vq] "v"(vq), [sq] "s"(sq), [vo] "v"(va), [sb] "s"(sa), [sc] "s"(sc), [nt] "s"(n8) : "s60", "vcc", "scc", "memory");
            }
            TS(6);
            v4u bt[4]; v2f rinv;
            {
                float M = fmaxf(fmaxf(asf(ps[0]), asf(ps[4])), fmaxf(asf(ps[8]), asf(ps[12])));
                M = x32max(x16max(M));
                float L = 0.f;
#pragma unroll
                for (int a = 0; a < 4; ++a) {
                    const float f = __builtin_amdgcn_exp2f(asf(ps[4 * a]) - M);
                    L += f * asf(ps[4 * a + 2]);
                    const u32 d = pk2(f, f);
                    bt[a] = v4u{d & k0, 0u, d & k2, 0u};
                }
                rinv[0] = rinv[1] = __builtin_amdgcn_rcpf(x32add(x16add(L)));
            }
            TS(7);
            {
                u32 t_;
#define CMB(D, i, B, C) "v_mfma_f32_16x16x32_bf16 v[" #D ":" #D "+3], " PV4(128, i) ", %[" #B "], " C "\n"
                asm volatile("s_mov_b32 s60, 0x10000\n" F_SLOW P_CHK8 P_LD8(-4096, -3840, 0, 256) "s_mov_b64 exec, -1\n s_waitcnt vmcnt(0)\n.Lpt_%=:\n"
                    "s_waitcnt vmcnt(6)\n" CMB(220, 0, b0, "0") F_A0(128) CMB(224, 1, b0, "0") F_A(128, 1)
                    "s_waitcnt vmcnt(4)\n" CMB(220, 2, b1, "v[220:223]") F_A(128, 2) CMB(224, 3, b1, "v[224:227]") F_A(128, 3)
                    "s_waitcnt vmcnt(2)\n" CMB(220, 4, b2, "v[220:223]") F_A(128, 4) CMB(224, 5, b2, "v[224:227]") F_A(128, 5)
                    "s_waitcnt vmcnt(0)\n" CMB(220, 6, b3, "v[220:223]") F_A(128, 6) CMB(224, 7, b3, "v[224:227]") F_A(128, 7) F_CMP F_BR
                    "s_nop 2\n v_pk_mul_f32 v[220:221], v[220:221], %[ri]\n v_pk_mul_f32 v[222:223], v[222:223], %[ri]\n"
                    "v_pk_mul_f32 v[224:225], v[224:225], %[ri]\n v_pk_mul_f32 v[226:227], v[226:227], %[ri]\n"
                    "v_cvt_pk_bf16_f32 v212, v220, v221\n v_cvt_pk_bf16_f32 v213, v222, v223\n v_cvt_pk_bf16_f32 v214, v224, v225\n v_cvt_pk_bf16_f32 v215, v226, v227\n"
                    "v_mov_b32_dpp v216, v212 row_ror:8" FULLM "v_mov_b32_dpp v217, v213 row_ror:8" FULLM
                    MF1(0) "v_mov_b32_dpp v218, v214 row_ror:8" FULLM "v_mov_b32_dpp v219, v215 row_ror:8" FULLM
                    MF1(1) MF1(2) MF1(3) MF1(4) MF1(5) MF1(6) MF1(7) GEMV2
                    : G_TMP(td, te), "+{v[128:159]}"(pa), [t] "=&v"(t_)
                    : "{v[64:95]}"(W0), "{v[96:127]}"(W1), [aw] "v"(aw), [ax] "v"(ax), [vo] "v"(va), [sb] "s"(sa), [sc] "s"(sc), [tag] "s"(tag), [nt] "s"(n16),
                      [b0] "v"(bt[0]), [b1] "v"(bt[1]), [b2] "v"(bt[2]), [b3] "v"(bt[3]), [ri] "v"(rinv) : F_CLOB);
            }
            TS(9);
            MARK(3);
        } else {
            const u32 tag = TAGC;
            const u32 ra = (u32)(uintptr_t)lds + L_G + (144u * (l & 3) + ((l >> 2) & 7)) * 4u;
            *(LAS u32*)(lds + L_PL) = (dloc > 128u ? 0u : 8192u) ^ ((xcd >> 2) << 13);
            bar();
            {
                const v2f r = redcols<16>(lds + L_R0, l);
                st8(ws + OFF_XQ + g * 16384 + (c >> 1) * 1024 + (c & 1) * 256, (u32)(l >> 5) * 8192 + (l & 31) * 8, v2u{pk2(r[0], r[1]), tag});
            }
            TS(4);
            bar();
            getpol();
            bar();
            {
                char* s0 = ws + (u32)__builtin_amdgcn_readfirstlane(OFF_XW + (c >> 1) * 32768 + (g >> 2) * 16384 + (g & 3) * 2048 + (c & 1) * 1024);
                v16u td; v4u o;
                asm volatile("v_mov_b32 v197, %[tag]\n v_mov_b32 v199, %[tag]\n s_barrier\n" RED8 "v_add_f32 v196, v180, v188\n v_add_f32 v198, v181, v189\n"
                    PUB4 : "=&{v[180:195]}"(td), "=&{v[196:199]}"(o) : RED_IN(ra), PUB_IN(s0, 131072), [tag] "s"(tag) : "memory");
            }
            TS(9);
            MARK(4);
        }
    } else if (idx >= 16) {
        const int s = idx - 16, g = AGRP(xcd, s), xq = (g - GROT) & 7, so = NEAR ? (s & 7) | (xcd >> 2 << 3) : s;
        {
            const u32 tag = TAGC;
            *(LAS v2u*)(lds + L_ST + (l & 15) * 8) = v2u{0u, 0u};
            v4u pq[2];
            v4u up;
            const char* sp = ws + OFF_PL + xcd * 1024;
            wait_until(t0, T_PA);
            asm volatile("v_not_b32 v177, %1\n global_load_dwordx4 v[176:179], %2, %3 sc1" : "=&{v[176:179]}"(up) : "s"(tag), "v"(0u), "s"(sp) : "memory");
            wait_until(t0, T_QP);
            {
                u32 d0, d1, e0, e1;
                asm volatile("v_readfirstlane_b32 %2, v177\n v_readfirstlane_b32 %3, v179\n v_readfirstlane_b32 %0, v176\n v_readfirstlane_b32 %1, v178"
                             : "=s"(d0), "=s"(d1), "=s"(e0), "=s"(e1) : "{v[176:179]}"(up));
                pol = (d0 > d1 || e0 != tag || e1 != tag) ? 0u : 8192u;
            }
            {
                const char* sb = ws + OFF_XQ + g * 16384 + CH(xcd, 0);
                _Pragma("unroll 1") for (int spin = 0; spin < SPIN_MAX; ++spin) {
                    TS(13);
                    asm volatile("global_load_dwordx4 v[176:179], %1, %2 sc1" : "+{v[176:179]}"(up) : "v"(0u), "s"(sp) : "memory");
                    ld16<0>(pq[0], sb, voh); ld16<2048>(pq[1], sb, voh);
                    wvm<1>(pq[0]); wvm<0>(pq[1]);
                    if (__all((badtag(pq[0], tag) | badtag(pq[1], tag)) == 0)) break;
                    TS(14);
                    if (QSL) __builtin_amdgcn_s_sleep(QSL);
                }
                LAS char* q = lds + L_Q + (l >> 5) * 320 + (l & 31) * 8;
                *(LAS v2u*)q = v2u{pq[0].x, pq[0].z};
                *(LAS v2u*)(q + 640) = v2u{pq[1].x, pq[1].z};
            }
            TS(3);
            bar();
            asm volatile("" : "+{v[176:179]}"(up));
            if (__builtin_amdgcn_readfirstlane(badtag(up, tag)) == 0) {
                pol = __builtin_amdgcn_readfirstlane(up.x) > __builtin_amdgcn_readfirstlane(up.z) ? 0u : 8192u;
                *(LAS u32*)(lds + L_PL) = pol;
                TS(15);
            } else getpol();
            {
                const u32 vo = (u32)(l >> 2) * 3584u + (l & 3) * 128, sp = 32 * s * 3584 + ((7 * xcd) >> 1) * 128;
                pf2((const char*)wg + sp, vo, vo + 16 * 3584, dump);
                pf2((const char*)wu + sp, vo, vo + 16 * 3584, dump);
            }
            TS(6);
            {
                v4u o; v2u st; u32 t_, f_;
                asm volatile("v_mov_b32 v181, %[tag]\n v_mov_b32 v183, %[tag]\n s_setprio 3\n.Lst_%=:\n ds_read_b64 v[184:185], %[ad]\n s_waitcnt lgkmcnt(0)\n"
                    "v_cmp_eq_u32 vcc, 0, v185\n v_max_f32_dpp %[t], v184, v184 quad_perm:[1,0,3,2]" FULLM "s_cbranch_vccnz .Lst_%=\n s_nop 0\n"
                    "v_max_f32_dpp v180, %[t], %[t] quad_perm:[2,3,0,1]" FULLM "v_sub_f32 %[f], v184, v180\n v_exp_f32 %[f], %[f]\n s_nop 0\n"
                    "v_mul_f32 %[t], %[f], v185\n ds_write_b32 %[af], %[f]\n s_nop 0\n v_add_f32_dpp %[t], %[t], %[t] quad_perm:[1,0,3,2]" FULLM "s_nop 1\n"
                    "v_add_f32_dpp v182, %[t], %[t] quad_perm:[2,3,0,1]" FULLM "s_mov_b64 exec, 0x1111\n global_store_dwordx4 %[vo], v[180:183], %[so] sc1\n s_mov_b64 exec, -1\n"
                    "s_waitcnt lgkmcnt(0)\n s_barrier\n s_setprio 0"
                    : "=&{v[180:183]}"(o), "=&{v[184:185]}"(st), [t] "=&v"(t_), [f] "=&v"(f_)
                    : [ad] "v"((u32)(uintptr_t)lds + L_ST + (l & 15) * 8), [af] "v"((u32)(uintptr_t)lds + L_F + (l & 3) * 16 + ((l >> 2) & 3) * 4), [vo] "v"((u32)l * 4), [tag] "s"(tag),
                      [so] "s"(ws + OFF_XS + CQ(xq) + (so >> 2) * 1024 + (so & 3) * 128) : "vcc", "memory");
            }
            TS(5);
            if (s < 4) {
                const int f = xcd * 4 + s, k = l & 7, pg = l >> 3;
                const int cl = 8 * (8 * (f & 1) + k) + 2 * ((f & 7) >> 1), col = 128 * (f >> 3) + cl;
                const u32 co = (cl >> 6) * 1024 + (cl & 63) * 8;
                const u32 xr = *(const GAS u32*)(xin + col), nep = NEXT(epc());
                pf1((const char*)out, 0u, dump);
                float ga0, ga1, gb0, gb1, a0, a1;
                {
                    v4u v0, v1;
                    const char* sb = ws + OFF_XR + CH(xcd, 65536) + (f >> 3) * 16384 + 4096;
                    _Pragma("unroll 1") for (int spin = 0; spin < SPIN_MAX; ++spin) {
                        ld16<-4096>(v0, sb, co); ld16<2048>(v1, sb, co);
                        wvm<1>(v0); wvm<0>(v1);
                        if (__all((badtag(v0, tag) | badtag(v1, tag)) == 0)) break;
                        __builtin_amdgcn_s_sleep(4);
                    }
                    ga0 = rbf(asf(v0.x)); ga1 = rbf(asf(v0.z)); gb0 = rbf(asf(v1.x)); gb1 = rbf(asf(v1.z));
                }
                TS(8);
                {
                    v4u v0;
                    const char* sb = ws + OFF_XW + CH(xcd, 131072) + (f >> 3) * 32768;
                    _Pragma("unroll 1") for (int spin = 0; spin < SPIN_MAX; ++spin) {
                        ld16<0>(v0, sb, (pg >> 2) * 16384 + (pg & 3) * 2048 + co);
                        wvm<0>(v0);
                        if (__all(badtag(v0, tag) == 0)) break;
                        __builtin_amdgcn_s_sleep(4);
                    }
                    a0 = asf(v0.x); a1 = asf(v0.z);
                }
                a0 += dpp<ROR(8)>(a0); a1 += dpp<ROR(8)>(a1); a0 = x32add(x16add(a0)); a1 = x32add(x16add(a1));
                float x20 = rbf(lo16(xr) + rbf(ga0 * rbf(a0))), x21 = rbf(hi16(xr) + rbf(ga1 * rbf(a1)));
                u32 ocol = col * 2;
                asm volatile("" : "+v"(x20), "+v"(x21), "+v"(gb0), "+v"(gb1), "+v"(ocol));
                TS(9);
                float y0, y1;
                v4u pd[7];
                {
                    const char* sb = ws + OFF_XD + CH(xcd & 4, 0) + (f & 15) * 32768 + 4096;
                    POLL7(pd, voh, sb, tag, -4096, -2048, 0, 2048, -4096, -2048, 0);
                    y0 = ((asf(pd[0].x) + asf(pd[1].x)) + (asf(pd[2].x) + asf(pd[3].x))) + ((asf(pd[4].x) + asf(pd[5].x)) + asf(pd[6].x));
                    y1 = ((asf(pd[0].z) + asf(pd[1].z)) + (asf(pd[2].z) + asf(pd[3].z))) + ((asf(pd[4].z) + asf(pd[5].z)) + asf(pd[6].z));
                }
                TS(10);
                y0 += dpp<ROR(8)>(y0); y1 += dpp<ROR(8)>(y1); y0 = x32add(x16add(y0)); y1 = x32add(x16add(y1));
                const u32 o = pk2(x20 + rbf(gb0 * rbf(y0)), x21 + rbf(gb1 * rbf(y1)));
                if (l < 8) __hip_atomic_store((GAS u32*)((char*)out + ocol), o, __ATOMIC_RELAXED, __HIP_MEMORY_SCOPE_AGENT);
                if (f == 0 && l == 0) __hip_atomic_store((GAS u32*)(ws + OFF_EP), nep, __ATOMIC_RELAXED, __HIP_MEMORY_SCOPE_AGENT);
                TS(11);
                asm volatile(KEND : : KEEP7(pd) : "memory");
                MARK(6);
            }
        }
    }
    TSDUMP();
#define VL(i, S, OFF) "global_load_dwordx4 v[4*" #i ":4*" #i "+3], v247, s[" #S ":" #S "+1] offset:" #OFF " nt\n"
#define VW "s_waitcnt vmcnt(6)\n"
#define KR1(T, t) "ds_read_b128 v[128+16*" #T "+4*" #t ":131+16*" #T "+4*" #t "], v[248+" #t "] offset:4096*" #T "\n"
#define KR(T) KR1(T, 0) KR1(T, 1) KR1(T, 2) KR1(T, 3)
#define PW1(B, R, j, p, S) "v_perm_b32 v[" #B "+4*" #j "+" #p "], v[" #R "+8*" #p "+4+(" #j ">>1)], v[" #R "+8*" #p "+(" #j ">>1)], s" #S "\n"
#define PW(B, R, p) PW1(B, R, 0, p, 48) PW1(B, R, 1, p, 49) PW1(B, R, 2, p, 48) PW1(B, R, 3, p, 49) PW1(B, R, 4, p, 48) PW1(B, R, 5, p, 49) PW1(B, R, 6, p, 48) PW1(B, R, 7, p, 49)
#define PV1ST "s_waitcnt vmcnt(6)\n" PW(96, 32, 0) "s_waitcnt vmcnt(4)\n" PW(96, 32, 1) "s_waitcnt vmcnt(2)\n" PW(96, 32, 2) "s_waitcnt vmcnt(0)\n" PW(96, 32, 3)
#define SM(T, t, C) "v_mfma_f32_16x16x32_bf16 v[208+4*" #T ":211+4*" #T "], v[128+16*" #T "+4*" #t ":131+16*" #T "+4*" #t "], v[192+4*" #t ":195+4*" #t "], " C "\n"
#define SA(T, t) SM(T, t, "v[208+4*" #T ":211+4*" #T "]")
#define MX(T) "v_max3_f32 v232, v232, v[208+4*" #T "], v[209+4*" #T "]\n v_max3_f32 v232, v232, v[210+4*" #T "], v[211+4*" #T "]\n"
#define RD(T, i) "v_cvt_pk_bf16_f32 v[208+4*" #T "+" #i "], 0, v[208+4*" #T "+" #i "]\n"
#define MG(T, i) "v_mul_f32_dpp v[224+" #i "], v[208+4*" #T "+" #i "], v246 quad_perm:[0,1,2,3] row_mask:0xf bank_mask:1<<" #T "\n"
#define R2(i) "v_cvt_pk_bf16_f32 v[224+" #i "], 0, v[224+" #i "]\n"
#define PM0(j) "v_mfma_f32_16x16x32_bf16 v[128+4*" #j ":131+4*" #j "], v[228:231], v[64+4*" #j ":67+4*" #j "], 0\n"
#define PM1(j) "v_mfma_f32_16x16x32_bf16 v[128+4*" #j ":131+4*" #j "], v[236:239], v[96+4*" #j ":99+4*" #j "], v[128+4*" #j ":131+4*" #j "]\n"
#define DW(j) "s_mov_b64 exec, s[58:59]\n ds_write_b128 v242, v[128+4*" #j ":131+4*" #j "] offset:256*" #j "\n s_mov_b64 exec, s[60:61]\n"
#define XBAR(n) "s_cbranch_execz .Lxb" #n "\n s_barrier\n.Lxb" #n ":\n"
#define ST(i)
#define LG(n) "s_waitcnt lgkmcnt(" #n ")\n"
#define STD
    asm volatile("s_endpgm\n.Ls3:\n v_xor_b32 v249, 64, v248\n v_xor_b32 v250, 0x80, v248\n v_xor_b32 v251, 0xc0, v248\n s_mov_b32 s48, 0x05040100\n s_mov_b32 s49, 0x07060302\n"
        "v_mov_b32 v246, 0x3db504f3\n s_mov_b32 s56, 0x3fb8aa3b\n s_mov_b32 s57, 0x3fb8aa3b\n"
        VW VL(0, 40, 0) VL(1, 40, 1024) KR(0) KR(1) VW VL(2, 40, 2048) VL(3, 40, 3072) KR(2) VW VL(4, 42, 0) VL(5, 42, 1024) VW VL(6, 42, 2048) VL(7, 42, 3072) KR(3)
        VW VL(8, 44, 0) VL(9, 44, 1024) PW(64, 0, 0) VW VL(10, 44, 2048) VL(11, 44, 3072) PW(64, 0, 1) VW VL(12, 46, 0) VL(13, 46, 1024) PW(64, 0, 2)
        VW VL(14, 46, 2048) VL(15, 46, 3072) "s_memrealtime s[62:63]\n" ST(1) PW(64, 0, 3)
        "s_waitcnt lgkmcnt(0)\n s_sub_u32 s62, s62, s50\n s_cmp_lt_u32 s62, " STR(T_L) "\n s_cselect_b32 s62, s67, 1\n s_cbranch_scc0 .Lla\n" PV1ST ".Lla:\n"
        "s_mul_i32 s51, s51, 1664525\n s_add_u32 s51, s51, 1013904223\n s_and_b32 s51, s51, 0xbfffbfff\n s_add_u32 s52, s52, s65\n s_addc_u32 s53, s53, 0\n" ST(2)
        XBAR(0) ST(3) "ds_read_b128 v[192:195], v240\n ds_read_b128 v[196:199], v240 offset:64\n ds_read_b128 v[200:203], v240 offset:128\n ds_read_b128 v[204:207], v240 offset:192\n"
        LG(3) SM(0, 0, "0") LG(2) SA(0, 1) LG(1) SA(0, 2) LG(0) SA(0, 3)
        SM(1, 0, "0") "s_nop 2\n" SA(1, 1) "s_nop 2\n" SA(1, 2) "v_max3_f32 v232, v208, v209, v210\n v_max_f32 v232, v232, v211\n" RD(0, 0)
        SA(1, 3) RD(0, 1) RD(0, 2) RD(0, 3) SM(2, 0, "0") MG(0, 0) MG(0, 1) MG(0, 2) SA(2, 1) MG(0, 3) MX(1) SA(2, 2) RD(1, 0) RD(1, 1) RD(1, 2)
        SA(2, 3) RD(1, 3) MG(1, 0) MG(1, 1) SM(3, 0, "0") MG(1, 2) MG(1, 3) "s_nop 0\n" SA(3, 1) MX(2) RD(2, 0) SA(3, 2) RD(2, 1) RD(2, 2) RD(2, 3)
        SA(3, 3) MG(2, 0) MG(2, 1) MG(2, 2) MG(2, 3) "s_nop 3\n"
        MX(3) RD(3, 0) "v_mov_b32 v233, v232\n" RD(3, 1) RD(3, 2) "v_permlane16_swap_b32 v232, v233\n" RD(3, 3) "v_max_f32 v232, v232, v233\n" MG(3, 0)
        "v_mov_b32 v233, v232\n" MG(3, 1) MG(3, 2) "v_permlane32_swap_b32 v232, v233\n" MG(3, 3) "v_max_f32 v232, v232, v233\n" R2(0)
        "v_mul_f32 v232, 0x3e0293ee, v232\n" R2(1) "v_ceil_f32 v232, v232\n" R2(2) R2(3)
        "v_pk_fma_f32 v[224:225], v[224:225], s[56:57], v[232:233] op_sel_hi:[1,1,0] neg_lo:[0,0,1] neg_hi:[0,0,1]\n"
        "v_pk_fma_f32 v[226:227], v[226:227], s[56:57], v[232:233] op_sel_hi:[1,1,0] neg_lo:[0,0,1] neg_hi:[0,0,1]\n"
        "v_exp_f32 v224, v224\n v_exp_f32 v225, v225\n v_exp_f32 v226, v226\n v_cvt_pk_bf16_f32 v228, v224, v225\n v_exp_f32 v227, v227\n"
        "v_add_f32 v233, v224, v225\n v_cvt_pk_bf16_f32 v229, v226, v227\n v_mov_b32_dpp v230, v228 row_shl:4" FULLM "v_add_f32 v234, v226, v227\n"
        "v_mov_b32_dpp v231, v229 row_shl:4" FULLM "v_add_f32 v233, v233, v234\n v_mov_b32_dpp v236, v228 row_shl:8" FULLM ST(4)
        PM0(0) "v_add_f32_dpp v233, v233, v233 row_ror:4" FULLM "v_mov_b32_dpp v237, v229 row_shl:8" FULLM "v_mov_b32_dpp v238, v228 row_shl:12" FULLM
        PM0(1) "v_add_f32_dpp v233, v233, v233 row_ror:8" FULLM "v_mov_b32_dpp v239, v229 row_shl:12" FULLM "s_nop 0\n"
        PM0(2) "v_mov_b32 v234, v233\n s_nop 1\n" PM0(3) "v_permlane16_swap_b32 v233, v234\n s_nop 0\n v_add_f32 v233, v233, v234\n"
        PM0(4) "v_mov_b32 v234, v233\n s_nop 1\n" PM0(5) "v_permlane32_swap_b32 v233, v234\n s_nop 0\n v_add_f32 v233, v233, v234\n"
        PM0(6) "ds_write_b64 v241, v[232:233]\n s_cmp_eq_u32 s62, 0\n" PM0(7) "s_cbranch_scc1 .Lea\n" PV1ST "s_nop 1\n.Lea:\n" ST(5)
        PM1(0) "s_nop 2\n" PM1(1) "s_nop 2\n" PM1(2) DW(0) PM1(3) DW(1) PM1(4) DW(2) PM1(5) DW(3) PM1(6) DW(4) PM1(7) DW(5) "s_nop 0\n" DW(6) "s_nop 0\n" DW(7)
        "s_cmp_lt_u32 s64, 2\n v_mov_b32 v229, s51\n v_mov_b32 v231, s51\n" ST(6) "s_waitcnt lgkmcnt(0)\n" XBAR(1) "s_cbranch_scc0 .Lsk\n"
        "ds_read_b128 v[192:195], v243\n ds_read_b128 v[208:211], v244\n ds_read_b128 v[196:199], v243 offset:16384\n ds_read_b128 v[212:215], v244 offset:16\n"
        "ds_read_b128 v[200:203], v243 offset:32768\n ds_read_b128 v[216:219], v244 offset:32\n ds_read_b128 v[204:207], v243 offset:49152\n ds_read_b128 v[220:223], v244 offset:48\n"
        "ds_read_b32 v224, v244 offset:%0\n"
        "s_waitcnt lgkmcnt(7)\n v_pk_mul_f32 v[192:193], v[192:193], v[208:209]\n v_pk_mul_f32 v[194:195], v[194:195], v[210:211]\n"
        "s_waitcnt lgkmcnt(5)\n v_pk_fma_f32 v[192:193], v[196:197], v[212:213], v[192:193]\n v_pk_fma_f32 v[194:195], v[198:199], v[214:215], v[194:195]\n"
        "s_waitcnt lgkmcnt(3)\n v_pk_fma_f32 v[192:193], v[200:201], v[216:217], v[192:193]\n v_pk_fma_f32 v[194:195], v[202:203], v[218:219], v[194:195]\n"
        "s_waitcnt lgkmcnt(1)\n v_pk_fma_f32 v[192:193], v[204:205], v[220:221], v[192:193]\n v_pk_fma_f32 v[194:195], v[206:207], v[222:223], v[194:195]\n"
        "s_waitcnt lgkmcnt(0)\n v_xor_b32 v224, v224, v245\n v_cvt_pk_bf16_f32 v228, v192, v193\n v_cvt_pk_bf16_f32 v230, v194, v195\n"
        "global_store_dwordx4 v224, v[228:231], s[54:55] sc1\n" ST(7) "s_cbranch_execz .Ls3r\n" STD "s_endpgm\n"
        ".Lsk:\n s_mov_b32 m0, s66\n s_sleep " STR(SKD) "\n global_load_lds_dword v252, s[52:53]\n" ST(7) STD "s_endpgm\n"
        ".Ls3r:\n s_setpc_b64 s[68:69]\n.Lfk_5:" : : "n"(L_PL - L_F) : "v252", "memory");
}

extern "C" void kernel_launch(void* const* d_in, const int* in_sizes, int n_in, void* d_out, int out_size, void* d_ws, size_t ws_size, hipStream_t stream) {
    fused_kernel<<<256, 320, 0, stream>>>((char*)d_ws + (-(uintptr_t)d_ws & 0x1fffff), (const u16*)d_in[10], (const u16*)d_in[9], (const u16*)d_in[3], (const u16*)d_in[0], (const u16*)d_in[1],
        (const u16*)d_in[2], KA8, (const u16*)d_in[4], (const u16*)d_in[6], (const u16*)d_in[7], (const u16*)d_in[8] KARG);
}
```
